```python
import torch

def _softmax(x, axis=-1):
    return torch.softmax(x, dim=axis)
def _gelu(x):
    return torch.nn.functional.gelu(x, approximate="tanh")
def _silu(x):
    return torch.nn.functional.silu(x)
def _sigmoid(x):
    return torch.sigmoid(x)
def _layernorm(x, axis=-1, eps=1e-5):
    m = x.mean(dim=axis, keepdim=True)
    v = x.var(dim=axis, keepdim=True, unbiased=False)
    return (x - m) / torch.sqrt(v + eps)
def _scatter_add(tgt, idx, src, axis=0):
    return tgt.clone().index_add_(axis, idx, src)
def _segment_sum(src, seg, n):
    out = torch.zeros((n,) + src.shape[1:], dtype=src.dtype)
    return out.index_add_(0, seg, src)
def _pad(x, pw):
    flat = tuple(v for pair in reversed(pw) for v in pair)
    return torch.nn.functional.pad(x, flat)

kernel_name = 'concat_heads__mha_block_cached__gqa_block_cached__near_ridge'

def setup_inputs():

    _w = (lambda a: (lambda t: t.to(torch.bfloat16) if t.is_floating_point() else t)(torch.as_tensor(a)))
    def _A_setup():
        B, Sq, D = 8, 144, 640
        H, Sk, Dh = 32, 512, 128
        return {
            "x": _w((lambda *s: torch.randn(*s, dtype=torch.bfloat16) * 0.8)(B, Sq, D)),
            "k_cache": _w((lambda *s: torch.randn(*s, dtype=torch.bfloat16) * 0.8)(B, H, Sk, Dh)),
            "v_cache": _w((lambda *s: torch.randn(*s, dtype=torch.bfloat16) * 0.05)(B, H, Sk, Dh)),
            "wq": _w((lambda *s: torch.randn(*s, dtype=torch.bfloat16) * 0.8)(D, H * Dh) * (D ** -0.5)),
            "wo": _w((lambda *s: torch.randn(*s, dtype=torch.bfloat16) * 0.05)(H * Dh, D) * ((H * Dh) ** -0.5)),
        }
    _A_in = _A_setup()

    def _B_setup():
        B, Sq, D = 8, 144, 640
        Hq, Hkv, Sk, Dh = 32, 32, 512, 128
        assert Hq % Hkv == 0, (Hq, Hkv)
        return {
            "x": _w((lambda *s: torch.randn(*s, dtype=torch.bfloat16) * 0.8)(B, Sq, D)),
            "k_cache": _w((lambda *s: torch.randn(*s, dtype=torch.bfloat16) * 0.8)(B, Hkv, Sk, Dh)),
            "v_cache": _w((lambda *s: torch.randn(*s, dtype=torch.bfloat16) * 0.05)(B, Hkv, Sk, Dh)),
            "wq": _w((lambda *s: torch.randn(*s, dtype=torch.bfloat16) * 0.8)(D, Hq * Dh) * (D ** -0.5)),
            "wo": _w((lambda *s: torch.randn(*s, dtype=torch.bfloat16) * 0.05)(Hq * Dh, D) * ((Hq * Dh) ** -0.5)),
        }
    _B_in = _B_setup()

    return {
        "A_x": _A_in["x"],
        "A_k_cache": _A_in["k_cache"],
        "A_v_cache": _A_in["v_cache"],
        "A_wq": _A_in["wq"],
        "A_wo": _A_in["wo"],
        "B_x": _B_in["x"],
        "B_k_cache": _B_in["k_cache"],
        "B_v_cache": _B_in["v_cache"],
        "B_wq": _B_in["wq"],
        "B_wo": _B_in["wo"],
    }

def reference(A_x, A_k_cache, A_v_cache, A_wq, A_wo, B_x, B_k_cache, B_v_cache, B_wq, B_wo):
    def _A_ref(x, k_cache, v_cache, wq, wo):
        B, Sq, D = x.shape
        H, Dh = 32, 128
        q = (lambda x, *d: x.permute(*d))((x @ wq).reshape(B, Sq, H, Dh), 0, 2, 1, 3)
        scores = torch.einsum("bhqd,bhkd->bhqk", q, k_cache) * (Dh ** -0.5)
        p = _softmax(scores, axis=-1)
        o = torch.einsum("bhqk,bhkd->bhqd", p, v_cache)
        return (lambda x, *d: x.permute(*d))(o, 0, 2, 1, 3).reshape(B, Sq, H * Dh) @ wo

    def _B_ref(x, k_cache, v_cache, wq, wo):
        B, Sq, D = x.shape
        Hq, Dh = 32, 128
        Hkv = k_cache.shape[1]
        rep = Hq // Hkv
        q = (lambda x, *d: x.permute(*d))((x @ wq).reshape(B, Sq, Hq, Dh), 0, 2, 1, 3)
        k = (lambda x, n, axis: torch.repeat_interleave(x, n, dim=axis))(k_cache, rep, axis=1)
        v = (lambda x, n, axis: torch.repeat_interleave(x, n, dim=axis))(v_cache, rep, axis=1)
        scores = torch.einsum("bhqd,bhkd->bhqk", q, k) * (Dh ** -0.5)
        p = _softmax(scores, axis=-1)
        o = torch.einsum("bhqk,bhkd->bhqd", p, v)
        return (lambda x, *d: x.permute(*d))(o, 0, 2, 1, 3).reshape(B, Sq, Hq * Dh) @ wo

    a = _A_ref(A_x, A_k_cache, A_v_cache, A_wq, A_wo)
    b = _B_ref(B_x, B_k_cache, B_v_cache, B_wq, B_wo)
    return torch.concatenate([a, b], axis=-1)
```

```cpp
#include <hip/hip_runtime.h>
#include <hip/hip_bf16.h>
#include <stdint.h>

typedef __attribute__((ext_vector_type(8))) short bf16x8;
typedef __attribute__((ext_vector_type(4))) short s16x4;
typedef __attribute__((ext_vector_type(4))) float f32x4;
typedef __attribute__((ext_vector_type(2))) float f32x2;
typedef __attribute__((ext_vector_type(2))) __bf16 bf16x2_t;
typedef __attribute__((ext_vector_type(4))) unsigned u32x4;
typedef __attribute__((ext_vector_type(2))) unsigned u32x2;
typedef unsigned short u16;
typedef __attribute__((address_space(3))) bf16x8 lds_bf16x8;
typedef __attribute__((address_space(3))) s16x4 lds_s16x4;
typedef __attribute__((address_space(3))) f32x4 lds_f32x4;
typedef __attribute__((address_space(3))) u32x4 lds_u32x4;
typedef __attribute__((address_space(3))) u32x2 lds_u32x2;
typedef __attribute__((address_space(3))) float lds_f32;
typedef __attribute__((address_space(3))) void lds_void;
typedef __amdgpu_buffer_rsrc_t rsrc_t;

#define DI __device__ __forceinline__
template <class T> DI T* lp(unsigned a) { return (T*)(uintptr_t)a; }

constexpr int NB = 8, SQ = 144, DM = 640, NH = 32, SK = 512, DH = 128;
constexpr int HD = NH * DH;
constexpr int MROWS = NB * SQ;
constexpr float QSCALE = 0.088388347648318440f * 1.4426950408889634f;
constexpr float THR = 8.0f;

DI unsigned cvtpk(float lo, float hi) {
  f32x2 v = {lo, hi};
  bf16x2_t b = __builtin_convertvector(v, bf16x2_t);
  return __builtin_bit_cast(unsigned, b);
}
DI f32x4 mfma16(bf16x8 a, bf16x8 b, f32x4 c) { return __builtin_amdgcn_mfma_f32_16x16x32_bf16(a, b, c, 0, 0, 0); }
DI rsrc_t mkrsrc(const void* p, unsigned bytes) { return __builtin_amdgcn_make_buffer_rsrc((void*)p, (short)0, (int)bytes, 0x00020000); }
DI void dma16(rsrc_t r, unsigned lds_addr, unsigned voff, unsigned soff) {
  __builtin_amdgcn_raw_ptr_buffer_load_lds(r, (__attribute__((address_space(3))) void*)(uintptr_t)lds_addr, 16, (int)voff, (int)soff, 0, 0);
}
DI bf16x8 lds_rd16(unsigned addr) { return *lp<lds_bf16x8>(addr); }
DI bf16x8 lds_tr2(unsigned a0, unsigned a1) {
  s16x4 lo = __builtin_amdgcn_ds_read_tr16_b64_v4i16(lp<lds_s16x4>(a0));
  s16x4 hi = __builtin_amdgcn_ds_read_tr16_b64_v4i16(lp<lds_s16x4>(a1));
  return __builtin_shufflevector(lo, hi, 0, 1, 2, 3, 4, 5, 6, 7);
}
DI float xmax_groups(float x) {
  auto a = __builtin_amdgcn_permlane16_swap(__float_as_uint(x), __float_as_uint(x), false, false);
  x = fmaxf(__uint_as_float(a[0]), __uint_as_float(a[1]));
  auto b = __builtin_amdgcn_permlane32_swap(__float_as_uint(x), __float_as_uint(x), false, false);
  return fmaxf(__uint_as_float(b[0]), __uint_as_float(b[1]));
}
DI float xsum_groups(float x) {
  auto a = __builtin_amdgcn_permlane16_swap(__float_as_uint(x), __float_as_uint(x), false, false);
  x = __uint_as_float(a[0]) + __uint_as_float(a[1]);
  auto b = __builtin_amdgcn_permlane32_swap(__float_as_uint(x), __float_as_uint(x), false, false);
  return __uint_as_float(b[0]) + __uint_as_float(b[1]);
}
#define WAITV(n) asm volatile("s_waitcnt vmcnt(" #n ")" ::: "memory")
#define WAITL0() asm volatile("s_waitcnt lgkmcnt(0)" ::: "memory")
#define BARRIER() asm volatile("s_waitcnt lgkmcnt(0)\n\ts_barrier" ::: "memory")

constexpr unsigned L_K = 0, L_V = 32768, L_X = 65536, L_R = 32768;
constexpr int K1_LDS = 81920;

template <int OFF> DI bf16x8 gload_asm(const void* p) {
  bf16x8 r;
  asm volatile("global_load_dwordx4 %0, %1, off offset:%2" : "=v"(r) : "v"(p), "i"(OFF) : "memory");
  return r;
}

__global__ __launch_bounds__(256, 2)
void k_attn(const u16* __restrict__ xA, const u16* __restrict__ kA, const u16* __restrict__ vA, const u16* __restrict__ wqA,
            const u16* __restrict__ xB, const u16* __restrict__ kB, const u16* __restrict__ vB, const u16* __restrict__ wqB,
            u16* __restrict__ o_ws) {
  extern __shared__ __attribute__((aligned(16))) char smem[];
  const unsigned lb = (unsigned)(uintptr_t)smem;
  const int tid = threadIdx.x, lane = tid & 63;
  const int wid = __builtin_amdgcn_readfirstlane(tid >> 6);
  const int bid = blockIdx.x;
  const int xcd = bid & 7, idx = bid >> 3;
  const int h = xcd * 4 + (idx & 3), b = (idx >> 2) & 7, br = idx >> 5;
  const u16* xg = (br ? xB : xA) + (size_t)b * SQ * DM;
  const rsrc_t rW = mkrsrc((br ? wqB : wqA) + h * DH, (unsigned)(DM * HD * 2 - h * DH * 2));
  const rsrc_t rK = mkrsrc((br ? kB : kA) + (size_t)(b * NH + h) * SK * DH, SK * DH * 2);
  const rsrc_t rV = mkrsrc((br ? vB : vA) + (size_t)(b * NH + h) * SK * DH, SK * DH * 2);

  const int l15 = lane & 15, g = lane >> 4, q4 = l15 >> 2, p4 = lane & 3;

  const unsigned kvo = (unsigned)((lane & 7) * 256 + (lane >> 3) * 16);
  const unsigned vvo = (unsigned)((l15 >> 1) * 256 + g * 32 + (lane & 1) * 16);
  const unsigned wri = (unsigned)(l15 >> 1);
  const unsigned wvo = (8u * (wri >> 2) + (wri & 3)) * (HD * 2) + g * 32 + (lane & 1) * 16;
  auto load_k = [&](int t) {
#pragma unroll
    for (int i = 0; i < 8; ++i) {
      const unsigned pc = wid * 8 + i;
      dma16(rK, lb + L_K + pc * 1024, kvo, t * 32768 + (pc >> 1) * 2048 + (pc & 1) * 128);
    }
  };
  auto load_v = [&](int t) {
#pragma unroll
    for (int i = 0; i < 8; ++i) {
      const unsigned pc = wid * 8 + i;
      dma16(rV, lb + L_V + pc * 1024, vvo, t * 32768 + (pc >> 1) * 2048 + (pc & 1) * 128);
    }
  };
  auto load_w = [&](int i) {
#pragma unroll
    for (int j = 0; j < 4; ++j) {
      const unsigned pc = wid * 4 + j, rgt = pc >> 1;
      dma16(rW, lb + L_R + (i % 3) * 16384 + pc * 1024, wvo, (unsigned)(i * 64 + 16 * (rgt >> 1) + 4 * (rgt & 1)) * (HD * 2) + (pc & 1) * 128);
    }
  };

  load_k(0);

  const u16* xp0 = xg + (size_t)((2 * wid) * 16 + l15) * DM + 8 * g;
  const u16* xp1 = xg + (size_t)((2 * wid + 1) * 16 + l15) * DM + 8 * g;
  const u16* xp2 = xg + (size_t)(128 + l15) * DM + 8 * g;
  bf16x8 xf[2][3][2];
  f32x4 qa[8][2], qa9[2];
#pragma unroll
  for (int j = 0; j < 8; ++j) { qa[j][0] = f32x4{0, 0, 0, 0}; qa[j][1] = f32x4{0, 0, 0, 0}; }
  qa9[0] = f32x4{0, 0, 0, 0}; qa9[1] = f32x4{0, 0, 0, 0};
  unsigned wb[4];
#pragma unroll
  for (int jp = 0; jp < 4; ++jp)
    wb[jp] = lb + L_R + (unsigned)((g >> 1) * 4096 + (q4 + 4 * (g & 1)) * 32 + 8 * p4 + ((2 * jp + 2 * wid) & 7) * 256);
#define XLOAD(P, I) do { \
    xf[P][0][0] = gload_asm<(I) * 128>(xp0);      xf[P][0][1] = gload_asm<(I) * 128 + 64>(xp0); \
    xf[P][1][0] = gload_asm<(I) * 128>(xp1);      xf[P][1][1] = gload_asm<(I) * 128 + 64>(xp1); \
    xf[P][2][0] = gload_asm<(I) * 128>(xp2);      xf[P][2][1] = gload_asm<(I) * 128 + 64>(xp2); } while (0)
#define XWAIT(P, N) asm volatile("s_waitcnt vmcnt(" #N ") lgkmcnt(0)\n\ts_barrier" \
    : "+v"(xf[P][0][0]), "+v"(xf[P][0][1]), "+v"(xf[P][1][0]), "+v"(xf[P][1][1]), "+v"(xf[P][2][0]), "+v"(xf[P][2][1]) :: "memory")
#define QSTEP(I) do { \
    constexpr int P_ = (I) & 1; \
    if ((I) < 9) { XWAIT(P_, 4); } else { XWAIT(P_, 0); } \
    if ((I) < 9) XLOAD(P_ ^ 1, ((I) + 1 < 10 ? (I) + 1 : 0)); \
    if ((I) < 8) load_w((I) + 2); \
    _Pragma("unroll") for (int ks = 0; ks < 2; ++ks) { \
      _Pragma("unroll") for (int j = 0; j < 8; ++j) { \
        const unsigned a0 = wb[j >> 1] + ((I) % 3) * 16384 + ks * 8192 + (j & 1) * 256; \
        bf16x8 A = lds_tr2(a0, a0 + 2048); \
        qa[j][0] = mfma16(A, xf[P_][0][ks], qa[j][0]); \
        qa[j][1] = mfma16(A, xf[P_][1][ks], qa[j][1]); \
        if (j < 2) qa9[j] = mfma16(A, xf[P_][2][ks], qa9[j]); \
      } } \
    } while (0)
  load_w(0);
  XLOAD(0, 0);
  load_w(1);
  QSTEP(0); QSTEP(1); QSTEP(2); QSTEP(3); QSTEP(4); QSTEP(5); QSTEP(6); QSTEP(7);
  QSTEP(8);
  QSTEP(9);
#undef QSTEP
#undef XWAIT
#undef XLOAD
  BARRIER();
  load_v(0);

  bf16x8 qf[2][4];
  {
    const unsigned wreg = lb + L_X + wid * 4096;
    const unsigned wq_l = wreg + (unsigned)((l15 >> 3) * 2048 + (l15 & 7) * 16 + (g >> 1) * 128 + (g & 1) * 8);
    const unsigned rq_l = wreg + (unsigned)((l15 >> 3) * 2048 + (l15 & 7) * 16 + g * 128);
#pragma unroll
    for (int qb = 0; qb < 2; ++qb) {
#pragma unroll
      for (int j = 0; j < 8; ++j) {
        const unsigned db = (j + 2 * wid) & 7;
        u32x2 w = {cvtpk(qa[j][qb][0] * QSCALE, qa[j][qb][1] * QSCALE), cvtpk(qa[j][qb][2] * QSCALE, qa[j][qb][3] * QSCALE)};
        *lp<lds_u32x2>(wq_l + db * 256) = w;
      }
      WAITL0();
#pragma unroll
      for (int s = 0; s < 4; ++s) qf[qb][s] = lds_rd16(rq_l + s * 512);
      WAITL0();
    }
#pragma unroll
    for (int j = 0; j < 2; ++j) {
      u32x2 w = {cvtpk(qa9[j][0] * QSCALE, qa9[j][1] * QSCALE), cvtpk(qa9[j][2] * QSCALE, qa9[j][3] * QSCALE)};
      *lp<lds_u32x2>(wq_l + j * 256) = w;
    }
    BARRIER();
  }
  const unsigned q9b = lb + L_X + (unsigned)((l15 >> 3) * 2048 + (l15 & 7) * 16 + g * 128);

  f32x4 O[2][8], O9[8];
#pragma unroll
  for (int d = 0; d < 8; ++d) { O[0][d] = f32x4{0, 0, 0, 0}; O[1][d] = f32x4{0, 0, 0, 0}; O9[d] = f32x4{0, 0, 0, 0}; }
  float mr[3] = {-1e30f, -1e30f, -1e30f}, ls[3] = {0.f, 0.f, 0.f};
  unsigned kb4[4], vb4[4];
#pragma unroll
  for (int jj = 0; jj < 4; ++jj) {
    const unsigned rot = (unsigned)(((jj + wid) & 3) * 8192);
    kb4[jj] = lb + L_K + rot + (unsigned)((l15 >> 3) * 2048 + g * 128 + (l15 & 7) * 16);
    vb4[jj] = lb + L_V + rot + (unsigned)((g >> 1) * 2048 + (4 * (g & 1) + q4) * 32 + 8 * p4);
  }

  WAITV(8);
  BARRIER();

  f32x4 S[2][4], S9[2];
  bf16x8 P[2][2], P9;

#define QK_HALF(H) do { \
    _Pragma("unroll") for (int kb = 0; kb < 4; ++kb) { \
      S[0][kb] = f32x4{0, 0, 0, 0}; S[1][kb] = f32x4{0, 0, 0, 0}; \
      if ((H) == 0 && kb < 2) S9[kb] = f32x4{0, 0, 0, 0}; \
      _Pragma("unroll") for (int s = 0; s < 4; ++s) { \
        bf16x8 A = lds_rd16(kb4[2 * (H) + (kb >> 1)] + (kb & 1) * 4096 + s * 512); \
        S[0][kb] = mfma16(A, qf[0][s], S[0][kb]); \
        S[1][kb] = mfma16(A, qf[1][s], S[1][kb]); \
        if ((H) == 0 && kb < 2) S9[kb] = mfma16(A, lds_rd16(q9b + s * 4096), S9[kb]); \
      } } } while (0)

#define SM_HALF(H) do { \
    float mx[3]; \
    _Pragma("unroll") for (int qb = 0; qb < 2; ++qb) { \
      float m = S[qb][0][0]; \
      _Pragma("unroll") for (int kb = 0; kb < 4; ++kb) \
        _Pragma("unroll") for (int r = 0; r < 4; ++r) m = fmaxf(m, S[qb][kb][r]); \
      mx[qb] = xmax_groups(m); } \
    if ((H) == 0) { float m = S9[0][0]; \
      _Pragma("unroll") for (int kb = 0; kb < 2; ++kb) \
        _Pragma("unroll") for (int r = 0; r < 4; ++r) m = fmaxf(m, S9[kb][r]); \
      mx[2] = xmax_groups(m); } else mx[2] = -1e30f; \
    const bool need = (mx[0] - mr[0] > THR) || (mx[1] - mr[1] > THR) || (mx[2] - mr[2] > THR); \
    if (__any(need)) { \
      _Pragma("unroll") for (int qb = 0; qb < 3; ++qb) { \
        if ((H) == 0 || qb < 2) { \
          const float mn = fmaxf(mr[qb], mx[qb]); \
          const float al = __builtin_amdgcn_exp2f(mr[qb] - mn); \
          mr[qb] = mn; ls[qb] *= al; \
          _Pragma("unroll") for (int d = 0; d < 8; ++d) { \
            if (qb < 2) O[qb][d] *= al; else O9[d] *= al; } \
        } } } \
    _Pragma("unroll") for (int qb = 0; qb < 2; ++qb) { \
      float sum = 0.f; \
      _Pragma("unroll") for (int kb = 0; kb < 4; ++kb) \
        _Pragma("unroll") for (int r = 0; r < 4; ++r) { \
          const float p = __builtin_amdgcn_exp2f(S[qb][kb][r] - mr[qb]); S[qb][kb][r] = p; sum += p; } \
      ls[qb] += sum; \
      _Pragma("unroll") for (int jj = 0; jj < 2; ++jj) { \
        u32x4 w = {cvtpk(S[qb][2 * jj][0], S[qb][2 * jj][1]), cvtpk(S[qb][2 * jj][2], S[qb][2 * jj][3]), \
                   cvtpk(S[qb][2 * jj + 1][0], S[qb][2 * jj + 1][1]), cvtpk(S[qb][2 * jj + 1][2], S[qb][2 * jj + 1][3])}; \
        P[qb][jj] = __builtin_bit_cast(bf16x8, w); } } \
    if ((H) == 0) { float sum = 0.f; \
      _Pragma("unroll") for (int kb = 0; kb < 2; ++kb) \
        _Pragma("unroll") for (int r = 0; r < 4; ++r) { \
          const float p = __builtin_amdgcn_exp2f(S9[kb][r] - mr[2]); S9[kb][r] = p; sum += p; } \
      ls[2] += sum; \
      u32x4 w = {cvtpk(S9[0][0], S9[0][1]), cvtpk(S9[0][2], S9[0][3]), cvtpk(S9[1][0], S9[1][1]), cvtpk(S9[1][2], S9[1][3])}; \
      P9 = __builtin_bit_cast(bf16x8, w); } \
    } while (0)

#define PV_HALF(H) do { \
    _Pragma("unroll") for (int jj = 0; jj < 2; ++jj) { \
      _Pragma("unroll") for (int d = 0; d < 8; ++d) { \
        const unsigned a0 = vb4[2 * (H) + jj] + d * 256; \
        bf16x8 A = lds_tr2(a0, a0 + 4096); \
        O[0][d] = mfma16(A, P[0][jj], O[0][d]); \
        O[1][d] = mfma16(A, P[1][jj], O[1][d]); \
        if ((H) == 0 && jj == 0) O9[d] = mfma16(A, P9, O9[d]); \
      } } } while (0)

  for (int t = 0; t < 4; ++t) {
    QK_HALF(0);
    __builtin_amdgcn_sched_barrier(0);
    SM_HALF(0);
    __builtin_amdgcn_sched_barrier(0);
    QK_HALF(1);
    WAITV(0);
    BARRIER();
    if (t < 3) load_k(t + 1);
    PV_HALF(0);
    __builtin_amdgcn_sched_barrier(0);
    SM_HALF(1);
    __builtin_amdgcn_sched_barrier(0);
    PV_HALF(1);
    WAITV(0);
    BARRIER();
    if (t < 3) load_v(t + 1);
  }
#undef QK_HALF
#undef SM_HALF
#undef PV_HALF

  {
  int tz = threadIdx.x; asm volatile("" : "+v"(tz));
  const int tid = tz, lane = tz & 63, l15 = lane & 15, g = lane >> 4;
  ls[0] = xsum_groups(ls[0]); ls[1] = xsum_groups(ls[1]); ls[2] = xsum_groups(ls[2]);
#pragma unroll
  for (int d = 0; d < 8; ++d) *lp<lds_f32x4>(lb + (unsigned)((wid * 8 + d) * 1024) + lane * 16) = O9[d];
  if (g == 0) {
    *lp<lds_f32>(lb + 32768 + wid * 64 + l15 * 4) = mr[2];
    *lp<lds_f32>(lb + 32768 + 256 + wid * 64 + l15 * 4) = ls[2];
  }
  const unsigned L_O = lb + 36864;
#pragma unroll
  for (int qb = 0; qb < 2; ++qb) {
    const float il = 1.0f / ls[qb];
    const unsigned row = (unsigned)((2 * wid + qb) * 16 + l15);
#pragma unroll
    for (int d = 0; d < 8; ++d) {
      u32x2 w = {cvtpk(O[qb][d][0] * il, O[qb][d][1] * il), cvtpk(O[qb][d][2] * il, O[qb][d][3] * il)};
      *lp<lds_u32x2>(L_O + 256u * row + 16u * ((unsigned)(2 * d + (g >> 1)) ^ l15) + 8u * (g & 1)) = w;
    }
  }
  BARRIER();
  {
    float mw[4], lw[4], M = -1e30f;
#pragma unroll
    for (int w = 0; w < 4; ++w) { mw[w] = *lp<lds_f32>(lb + 32768 + w * 64 + l15 * 4); lw[w] = *lp<lds_f32>(lb + 32768 + 256 + w * 64 + l15 * 4); M = fmaxf(M, mw[w]); }
    float L = 0.f, sc[4];
#pragma unroll
    for (int w = 0; w < 4; ++w) { sc[w] = __builtin_amdgcn_exp2f(mw[w] - M); L += lw[w] * sc[w]; }
    const float il = 1.0f / L;
    const unsigned row = 128u + l15;
#pragma unroll
    for (int j = 0; j < 2; ++j) {
      const int d = 2 * wid + j;
      f32x4 a = {0, 0, 0, 0};
#pragma unroll
      for (int w = 0; w < 4; ++w) a += *lp<lds_f32x4>(lb + (unsigned)((w * 8 + d) * 1024) + lane * 16) * sc[w];
      u32x2 w2 = {cvtpk(a[0] * il, a[1] * il), cvtpk(a[2] * il, a[3] * il)};
      *lp<lds_u32x2>(L_O + 256u * row + 16u * ((unsigned)(2 * d + (g >> 1)) ^ l15) + 8u * (g & 1)) = w2;
    }
  }
  BARRIER();
  {
    u16* og = o_ws + ((size_t)br * MROWS + (size_t)b * SQ) * HD + h * DH;
#pragma unroll
    for (int i = 0; i < 9; ++i) {
      const unsigned row = (unsigned)(i * 16 + (tid >> 4)), c = tid & 15;
      u32x4 v = *lp<lds_u32x4>(L_O + 256u * row + 16u * (c ^ (row & 15)));
      *reinterpret_cast<u32x4*>(og + (size_t)row * HD + c * 8) = v;
    }
  }
  }
}

constexpr int K2_STAGE = 38912, K2_NST = 4, K2_LDS = K2_STAGE * K2_NST;

__global__ __launch_bounds__(256, 1)
void k_oproj(const u16* __restrict__ o_ws, const u16* __restrict__ woA, const u16* __restrict__ woB, float* __restrict__ part) {
  extern __shared__ __attribute__((aligned(16))) char smem[];
  const unsigned lb = (unsigned)(uintptr_t)smem;
  const int tid = threadIdx.x, lane = tid & 63;
  const int wid = __builtin_amdgcn_readfirstlane(tid >> 6);
  const int bid = blockIdx.x;
  const int xcd = bid & 7, idx = bid >> 3;
  const int br = xcd >> 2, ks = xcd & 3, b = idx >> 2, nt = idx & 3;
  const int nh = wid & 1, kh = wid >> 1;
  const rsrc_t rO = mkrsrc(o_ws + ((size_t)br * MROWS + (size_t)b * SQ) * HD + ks * 1024, (unsigned)(SQ * HD * 2 - ks * 2048));
  const rsrc_t rW = mkrsrc((br ? woB : woA) + (size_t)ks * 1024 * DM + nt * 160, (unsigned)(1024 * DM * 2 - nt * 320));
  const int l15 = lane & 15, g = lane >> 4, q4 = l15 >> 2, p4 = lane & 3;

  const unsigned ovo = (unsigned)((lane & 7) * (HD * 2) + (lane >> 3) * 16);
  const unsigned wri = (unsigned)(l15 >> 1);
  const unsigned wvo = (4u * (g >> 1) + 8u * (wri >> 2) + (wri & 3)) * (DM * 2) + (g & 1) * 32 + (lane & 1) * 16;
  auto load_stage = [&](int c) {
    const unsigned sb = lb + (c & 3) * K2_STAGE;
#pragma unroll
    for (int i = 0; i < 5; ++i) {
      unsigned pc = wid + 4 * i;
      if (pc > 17) pc -= 4;
      dma16(rO, sb + pc * 1024, ovo, pc * 8 * (HD * 2) + c * 128);
    }
#pragma unroll
    for (int i = 0; i < 5; ++i) {
      const unsigned pc = i * 4 + wid;
      dma16(rW, sb + 18432 + pc * 1024, wvo, (unsigned)(c * 64 + wid * 16) * (DM * 2) + i * 64);
    }
  };
  f32x4 acc[5][9];
#pragma unroll
  for (int n = 0; n < 5; ++n)
#pragma unroll
    for (int m = 0; m < 9; ++m) acc[n][m] = f32x4{0, 0, 0, 0};
  const unsigned boff = (unsigned)((l15 >> 3) * 1024 + (4 * kh + g) * 128 + (l15 & 7) * 16);
  unsigned aoff[5];
#pragma unroll
  for (int n = 0; n < 5; ++n) {
    const unsigned nb = 5 * nh + n;
    aoff[n] = 18432u + (nb >> 1) * 4096 + (nb & 1) * 256 + kh * 2048 + (g >> 1) * 1024 + (q4 + 4 * (g & 1)) * 32 + 8 * p4;
  }

  load_stage(0); load_stage(1); load_stage(2);
  for (int c = 0; c < 16; ++c) {
    if (c < 14) { WAITV(20); } else if (c < 15) { WAITV(10); } else { WAITV(0); }
    BARRIER();
    if (c + 3 < 16) load_stage(c + 3);
    const unsigned sb = lb + (c & 3) * K2_STAGE;
    bf16x8 Bf[9];
#pragma unroll
    for (int m = 0; m < 9; ++m) Bf[m] = lds_rd16(sb + boff + m * 2048);
#pragma unroll
    for (int n = 0; n < 5; ++n) {
      const unsigned a0 = sb + aoff[n];
      bf16x8 A = lds_tr2(a0, a0 + 512);
#pragma unroll
      for (int m = 0; m < 9; ++m) acc[n][m] = mfma16(A, Bf[m], acc[n][m]);
    }
  }
  BARRIER();
  if (kh == 1) {
#pragma unroll
    for (int n = 0; n < 5; ++n)
#pragma unroll
      for (int m = 0; m < 9; ++m) *lp<lds_f32x4>(lb + (unsigned)(((nh * 45) + n * 9 + m) * 1024) + lane * 16) = acc[n][m];
  }
  BARRIER();
  if (kh == 0) {
    float* pg = part + ((size_t)ks * 2 * MROWS + (size_t)br * MROWS + (size_t)b * SQ) * DM + nt * 160;
#pragma unroll
    for (int n = 0; n < 5; ++n)
#pragma unroll
      for (int m = 0; m < 9; ++m) {
        f32x4 v = acc[n][m] + *lp<lds_f32x4>(lb + (unsigned)(((nh * 45) + n * 9 + m) * 1024) + lane * 16);
        *reinterpret_cast<f32x4*>(pg + (size_t)(m * 16 + l15) * DM + (5 * nh + n) * 16 + 4 * g) = v;
      }
  }
}

__global__ __launch_bounds__(256)
void k_reduce(const float* __restrict__ part, u16* __restrict__ out) {
  const int i = blockIdx.x * 256 + threadIdx.x;
  constexpr int PER = 2 * MROWS * DM;
  const int e = i * 4;
  f32x4 a = *reinterpret_cast<const f32x4*>(part + e);
#pragma unroll
  for (int s = 1; s < 4; ++s) a += *reinterpret_cast<const f32x4*>(part + (size_t)s * PER + e);
  const int brm = e / DM, n = e - brm * DM, br = brm / MROWS, m = brm - br * MROWS;
  u32x2 w = {cvtpk(a[0], a[1]), cvtpk(a[2], a[3])};
  *reinterpret_cast<u32x2*>(out + (size_t)m * (2 * DM) + br * DM + n) = w;
}

extern "C" void kernel_launch(void* const* d_in, const int* in_sizes, int n_in,
                              void* d_out, int out_size, void* d_ws, size_t ws_size,
                              hipStream_t stream) {
  const u16* xA = (const u16*)d_in[0];
  const u16* kA = (const u16*)d_in[1];
  const u16* vA = (const u16*)d_in[2];
  const u16* wqA = (const u16*)d_in[3];
  const u16* woA = (const u16*)d_in[4];
  const u16* xB = (const u16*)d_in[5];
  const u16* kB = (const u16*)d_in[6];
  const u16* vB = (const u16*)d_in[7];
  const u16* wqB = (const u16*)d_in[8];
  const u16* woB = (const u16*)d_in[9];
  u16* o_ws = (u16*)d_ws;
  float* part = (float*)((char*)d_ws + (size_t)2 * MROWS * HD * 2);
  (void)hipFuncSetAttribute((const void*)k_attn, hipFuncAttributeMaxDynamicSharedMemorySize, K1_LDS);
  (void)hipFuncSetAttribute((const void*)k_oproj, hipFuncAttributeMaxDynamicSharedMemorySize, K2_LDS);
  k_attn<<<512, 256, K1_LDS, stream>>>(xA, kA, vA, wqA, xB, kB, vB, wqB, o_ws);
  k_oproj<<<256, 256, K2_LDS, stream>>>(o_ws, woA, woB, part);
  k_reduce<<<2 * MROWS * DM / 4 / 256, 256, 0, stream>>>(part, (u16*)d_out);
}
```

```cpp
#include <hip/hip_runtime.h>
#include <hip/hip_bf16.h>
#include <stdint.h>
#include <type_traits>

typedef __attribute__((ext_vector_type(8))) short bf16x8;
typedef __attribute__((ext_vector_type(4))) short s16x4;
typedef __attribute__((ext_vector_type(4))) float f32x4;
typedef __attribute__((ext_vector_type(2))) float f32x2;
typedef __attribute__((ext_vector_type(2))) __bf16 bf16x2_t;
typedef __attribute__((ext_vector_type(4))) unsigned u32x4;
typedef __attribute__((ext_vector_type(2))) unsigned u32x2;
typedef unsigned short u16;
typedef __attribute__((address_space(3))) bf16x8 lds_bf16x8;
typedef __attribute__((address_space(3))) s16x4 lds_s16x4;
typedef __attribute__((address_space(3))) f32x4 lds_f32x4;
typedef __attribute__((address_space(3))) u32x4 lds_u32x4;
typedef __attribute__((address_space(3))) u32x2 lds_u32x2;
typedef __attribute__((address_space(3))) float lds_f32;
typedef __amdgpu_buffer_rsrc_t rsrc_t;

#define DI __device__ __forceinline__
template <class T> DI T* lp(unsigned a) { return (T*)(uintptr_t)a; }

constexpr int NB = 8, SQ = 144, DM = 640, NH = 32, SK = 512, DH = 128;
constexpr int HD = NH * DH;
constexpr int MROWS = NB * SQ;
constexpr float QSCALE = 0.088388347648318440f * 1.4426950408889634f;
constexpr float THR = 8.0f;

DI unsigned cvtpk(float lo, float hi) {
  f32x2 v = {lo, hi};
  bf16x2_t b = __builtin_convertvector(v, bf16x2_t);
  return __builtin_bit_cast(unsigned, b);
}
DI f32x4 mfma16(bf16x8 a, bf16x8 b, f32x4 c) { return __builtin_amdgcn_mfma_f32_16x16x32_bf16(a, b, c, 0, 0, 0); }
DI rsrc_t mkrsrc(const void* p, unsigned bytes) { return __builtin_amdgcn_make_buffer_rsrc((void*)p, (short)0, (int)bytes, 0x00020000); }
DI void dma1(rsrc_t r, unsigned lds_addr, unsigned voff, unsigned soff) {
  asm volatile("s_mov_b32 m0, %0\n\ts_nop 0\n\tbuffer_load_dwordx4 %1, %2, %3 offen lds" :: "s"(lds_addr), "v"(voff), "s"(r), "s"(soff) : "memory", "m0");
}
DI void dma4(rsrc_t r, unsigned lds_addr, unsigned v0, unsigned v1, unsigned v2, unsigned v3, unsigned soff) {
  asm volatile("s_mov_b32 m0, %0\n\ts_nop 0\n\t"
               "buffer_load_dwordx4 %1, %5, %6 offen lds\n\t"
               "buffer_load_dwordx4 %2, %5, %6 offen offset:1024 lds\n\t"
               "buffer_load_dwordx4 %3, %5, %6 offen offset:2048 lds\n\t"
               "buffer_load_dwordx4 %4, %5, %6 offen offset:3072 lds"
               :: "s"(lds_addr), "v"(v0), "v"(v1), "v"(v2), "v"(v3), "s"(r), "s"(soff) : "memory", "m0");
}
DI void dma3(rsrc_t r, unsigned lds_addr, unsigned v0, unsigned soff) {
  asm volatile("s_mov_b32 m0, %0\n\ts_nop 0\n\t"
               "buffer_load_dwordx4 %1, %2, %3 offen lds\n\t"
               "buffer_load_dwordx4 %1, %2, %3 offen offset:1024 lds\n\t"
               "buffer_load_dwordx4 %1, %2, %3 offen offset:2048 lds"
               :: "s"(lds_addr), "v"(v0), "s"(r), "s"(soff) : "memory", "m0");
}
DI bf16x8 bload_asm(rsrc_t r, unsigned voff, unsigned soff) {
  bf16x8 v;
  asm volatile("buffer_load_dwordx4 %0, %1, %2, %3 offen" : "=v"(v) : "v"(voff), "s"(r), "s"(soff) : "memory");
  return v;
}
DI bf16x8 lds_rd16(unsigned addr) { return *lp<lds_bf16x8>(addr); }
DI bf16x8 lds_tr2(unsigned a0, unsigned a1) {
  s16x4 lo = __builtin_amdgcn_ds_read_tr16_b64_v4i16(lp<lds_s16x4>(a0));
  s16x4 hi = __builtin_amdgcn_ds_read_tr16_b64_v4i16(lp<lds_s16x4>(a1));
  return __builtin_shufflevector(lo, hi, 0, 1, 2, 3, 4, 5, 6, 7);
}
DI float xmax_groups(float x) {
  auto a = __builtin_amdgcn_permlane16_swap(__float_as_uint(x), __float_as_uint(x), false, false);
  x = fmaxf(__uint_as_float(a[0]), __uint_as_float(a[1]));
  auto b = __builtin_amdgcn_permlane32_swap(__float_as_uint(x), __float_as_uint(x), false, false);
  return fmaxf(__uint_as_float(b[0]), __uint_as_float(b[1]));
}
DI float xsum_groups(float x) {
  auto a = __builtin_amdgcn_permlane16_swap(__float_as_uint(x), __float_as_uint(x), false, false);
  x = __uint_as_float(a[0]) + __uint_as_float(a[1]);
  auto b = __builtin_amdgcn_permlane32_swap(__float_as_uint(x), __float_as_uint(x), false, false);
  return __uint_as_float(b[0]) + __uint_as_float(b[1]);
}
#define STAMP(i)
template <int N> DI void waitv() { asm volatile("s_waitcnt vmcnt(%0)" :: "i"(N) : "memory"); }
#define WAITL0() asm volatile("s_waitcnt lgkmcnt(0)" ::: "memory")
#define BARRIER() asm volatile("s_waitcnt lgkmcnt(0)\n\ts_barrier" ::: "memory")
#define SBAR() __builtin_amdgcn_sched_barrier(0)

constexpr int K1_LDS = 81920;
constexpr int QD = 5;
constexpr int q_dma(int j) { return (j >= QD && j < QD + 16 && ((j - QD) & 3) == 0) ? 4 : 0; }
constexpr int q_size(int j) { return (j < 20 ? 3 : 0) + (j + 1 < 20 ? 2 : 0) + q_dma(j); }
constexpr int q_wait(int i) { int n = q_dma(i); for (int j = i + 1; j < i + QD; ++j) n += q_size(j); return n; }

struct K1Q {
  bf16x8 xf[QD + 1][3];
  bf16x8 wf[QD + 1][2];
  f32x4 qa[8][2], qa9[2];
};

__global__ __launch_bounds__(256, 2)
void k_attn(const u16* __restrict__ xA, const u16* __restrict__ kA, const u16* __restrict__ vA, const u16* __restrict__ wqA,
            const u16* __restrict__ xB, const u16* __restrict__ kB, const u16* __restrict__ vB, const u16* __restrict__ wqB,
            u16* __restrict__ o_ws, unsigned long long* __restrict__ dbg) {
  extern __shared__ __attribute__((aligned(16))) char smem[];
  const unsigned lb = (unsigned)(uintptr_t)smem;
  const int tid = threadIdx.x, lane = tid & 63;
  const int wid = __builtin_amdgcn_readfirstlane(tid >> 6);
  const int bid = blockIdx.x;
  const int xcd = bid & 7, idx = bid >> 3;
  const int h = xcd * 4 + (idx & 3), b = (idx >> 2) & 7, br = idx >> 5;
  const rsrc_t rX = mkrsrc((br ? xB : xA) + (size_t)b * SQ * DM, SQ * DM * 2);
  const rsrc_t rW = mkrsrc((br ? wqB : wqA) + h * DH, (unsigned)(DM * HD * 2 - h * DH * 2));
  const rsrc_t rK = mkrsrc((br ? kB : kA) + (size_t)(b * NH + h) * SK * DH, SK * DH * 2);
  const rsrc_t rV = mkrsrc((br ? vB : vA) + (size_t)(b * NH + h) * SK * DH, SK * DH * 2);

  const int l15 = lane & 15, g = lane >> 4, q4 = l15 >> 2, p4 = lane & 3;

  unsigned kvo[4], vvo[2];
  {
    const unsigned s_ = lane >> 4, a_ = (lane >> 2) & 3, r_ = lane & 3;
    kvo[0] = r_ * 256 + (4 * s_ + (a_ ^ 0)) * 16; kvo[1] = r_ * 256 + (4 * s_ + (a_ ^ 2)) * 16;
    kvo[2] = r_ * 256 + (4 * s_ + (a_ ^ 3)) * 16; kvo[3] = r_ * 256 + (4 * s_ + (a_ ^ 1)) * 16;
    const unsigned d_ = lane >> 3, rv = (lane >> 1) & 3, hf = lane & 1;
    vvo[0] = rv * 256 + d_ * 32 + hf * 16; vvo[1] = rv * 256 + (d_ ^ 1) * 32 + hf * 16;
  }
  auto load_item = [&](int n, int slot) {
    const unsigned la = lb + slot * 16384 + wid * 4096, so = (unsigned)(n >> 1) * 16384 + wid * 4096;
    if (n & 1) dma4(rV, la, vvo[0], vvo[1], vvo[0], vvo[1], so);
    else       dma4(rK, la, kvo[0], kvo[1], kvo[2], kvo[3], so);
  };

  STAMP(0);
  K1Q Q;
  const unsigned xv0 = (unsigned)(((2 * wid) * 16 + l15) * DM * 2 + 16 * g);
  const unsigned xv2 = (unsigned)((128 + l15) * DM * 2 + 16 * g);
  const unsigned wri = (unsigned)(l15 >> 1);
  const unsigned wvo = (8u * (wri >> 2) + (wri & 3) + 16u * (wid >> 1) + 4u * (wid & 1)) * (HD * 2) + g * 32 + (lane & 1) * 16;
  const unsigned L_W = lb + 4 * 16384;
#pragma unroll
  for (int j = 0; j < 8; ++j) { Q.qa[j][0] = f32x4{0, 0, 0, 0}; Q.qa[j][1] = f32x4{0, 0, 0, 0}; }
  Q.qa9[0] = f32x4{0, 0, 0, 0}; Q.qa9[1] = f32x4{0, 0, 0, 0};
  unsigned wb[4];
#pragma unroll
  for (int jp = 0; jp < 4; ++jp)
    wb[jp] = L_W + (unsigned)((g >> 1) * 4096 + (q4 + 4 * (g & 1)) * 32 + 8 * p4 + ((2 * jp + 2 * wid) & 7) * 256);

  auto wload = [&](auto J) { constexpr int j = J; Q.wf[j % (QD + 1)][0] = bload_asm(rW, wvo, j * 32 * (HD * 2)); Q.wf[j % (QD + 1)][1] = bload_asm(rW, wvo, j * 32 * (HD * 2) + 128); };
  auto xload = [&](auto J) { constexpr int j = J;
    Q.xf[j % (QD + 1)][0] = bload_asm(rX, xv0, j * 64); Q.xf[j % (QD + 1)][1] = bload_asm(rX, xv0, j * 64 + 16 * DM * 2); Q.xf[j % (QD + 1)][2] = bload_asm(rX, xv2, j * 64); };
  auto group = [&](auto J) { constexpr int j = J;
    if constexpr (j < 20) xload(std::integral_constant<int, j>{});
    if constexpr (j + 1 < 20) wload(std::integral_constant<int, j + 1>{});
    if constexpr (q_dma(j) != 0) load_item((j - QD) >> 2, (j - QD) >> 2);
  };
  auto wwrite = [&](auto J) { constexpr int j = J;
    *lp<lds_bf16x8>(L_W + (j & 1) * 8192 + wid * 2048 + lane * 16) = Q.wf[j % (QD + 1)][0];
    *lp<lds_bf16x8>(L_W + (j & 1) * 8192 + wid * 2048 + 1024 + lane * 16) = Q.wf[j % (QD + 1)][1];
  };
  auto qstep = [&](auto I) { constexpr int i = I; constexpr int bx = i % (QD + 1), bw = (i + 1) % (QD + 1);
    if constexpr (i + 1 < 20)
      asm volatile("s_waitcnt vmcnt(%5) lgkmcnt(0)\n\ts_barrier" : "+v"(Q.xf[bx][0]), "+v"(Q.xf[bx][1]), "+v"(Q.xf[bx][2]), "+v"(Q.wf[bw][0]), "+v"(Q.wf[bw][1]) : "i"(q_wait(i)) : "memory");
    else
      asm volatile("s_waitcnt vmcnt(%3) lgkmcnt(0)\n\ts_barrier" : "+v"(Q.xf[bx][0]), "+v"(Q.xf[bx][1]), "+v"(Q.xf[bx][2]) : "i"(q_wait(i)) : "memory");
    if constexpr (i + 1 < 20) wwrite(std::integral_constant<int, i + 1>{});
    group(std::integral_constant<int, i + QD>{});
#pragma unroll
    for (int j = 0; j < 8; ++j) {
      const unsigned a0 = wb[j >> 1] + (i & 1) * 8192 + (j & 1) * 256;
      bf16x8 A = lds_tr2(a0, a0 + 2048);
      Q.qa[j][0] = mfma16(A, Q.xf[bx][0], Q.qa[j][0]);
      Q.qa[j][1] = mfma16(A, Q.xf[bx][1], Q.qa[j][1]);
      if (j < 2) Q.qa9[j] = mfma16(A, Q.xf[bx][2], Q.qa9[j]);
    }
  };
  wload(std::integral_constant<int, 0>{});
  group(std::integral_constant<int, 0>{}); group(std::integral_constant<int, 1>{}); group(std::integral_constant<int, 2>{});
  group(std::integral_constant<int, 3>{}); group(std::integral_constant<int, 4>{});
  {
    constexpr int n0 = q_size(0) + q_size(1) + q_size(2) + q_size(3) + q_size(4);
    asm volatile("s_waitcnt vmcnt(%2)" : "+v"(Q.wf[0][0]), "+v"(Q.wf[0][1]) : "i"(n0) : "memory");
    wwrite(std::integral_constant<int, 0>{});
  }
  STAMP(1);
#define QS(i) qstep(std::integral_constant<int, i>{})
  QS(0); QS(1); QS(2); QS(3); QS(4); STAMP(2); QS(5); QS(6); QS(7); QS(8); QS(9); STAMP(3);
  QS(10); QS(11); QS(12); QS(13); QS(14); STAMP(4); QS(15); QS(16); QS(17); QS(18); QS(19);
#undef QS
  STAMP(5);
  BARRIER();

  bf16x8 qf[3][4];
  {
    const unsigned wreg = L_W + wid * 4096;
    const unsigned wq_l = wreg + (unsigned)((l15 >> 3) * 2048 + (l15 & 7) * 16 + (g >> 1) * 128 + (g & 1) * 8);
    const unsigned rq_l = wreg + (unsigned)((l15 >> 3) * 2048 + (l15 & 7) * 16 + g * 128);
#pragma unroll
    for (int qb = 0; qb < 2; ++qb) {
#pragma unroll
      for (int j = 0; j < 8; ++j) {
        const unsigned db = (j + 2 * wid) & 7;
        u32x2 w = {cvtpk(Q.qa[j][qb][0] * QSCALE, Q.qa[j][qb][1] * QSCALE), cvtpk(Q.qa[j][qb][2] * QSCALE, Q.qa[j][qb][3] * QSCALE)};
        *lp<lds_u32x2>(wq_l + db * 256) = w;
      }
      WAITL0();
#pragma unroll
      for (int s = 0; s < 4; ++s) qf[qb][s] = lds_rd16(rq_l + s * 512);
      WAITL0();
    }
#pragma unroll
    for (int j = 0; j < 2; ++j) {
      u32x2 w = {cvtpk(Q.qa9[j][0] * QSCALE, Q.qa9[j][1] * QSCALE), cvtpk(Q.qa9[j][2] * QSCALE, Q.qa9[j][3] * QSCALE)};
      *lp<lds_u32x2>(wq_l + j * 256) = w;
    }
    BARRIER();
#pragma unroll
    for (int s = 0; s < 4; ++s) qf[2][s] = lds_rd16(rq_l - wid * 4096 + s * 4096);
    BARRIER();
  }
  load_item(4, 4);
  STAMP(6);

  f32x4 O[2][8], O9[8];
#pragma unroll
  for (int d = 0; d < 8; ++d) { O[0][d] = f32x4{0, 0, 0, 0}; O[1][d] = f32x4{0, 0, 0, 0}; O9[d] = f32x4{0, 0, 0, 0}; }
  float mr[3] = {-1e30f, -1e30f, -1e30f}, ls[3] = {0.f, 0.f, 0.f};
  unsigned kbase, vbase0, vbase1;
  {
    const unsigned p_ = l15 >> 2, sg = (0x1320u >> (4 * p_)) & 3;
    kbase = lb + p_ * 1024 + ((g ^ sg) * 4 + (l15 & 3)) * 16;
    vbase0 = lb + g * 1024 + ((0 ^ (g & 1)) << 7) + q4 * 32 + 8 * p4;
    vbase1 = lb + g * 1024 + ((1 ^ (g & 1)) << 7) + q4 * 32 + 8 * p4;
  }
  f32x4 S[2][4], S9[2];
  bf16x8 P[2][2], P9;
  int slot = 0;
  for (int t = 0; t < 8; ++t) {
    const bool act9 = ((wid >> 1) == (t & 1));
    if (t == 0) waitv<16>(); else if (t < 7) waitv<12>(); else waitv<4>();
    BARRIER();
    if (t >= 1 && t < 6) load_item(2 * t + 4, slot == 0 ? 4 : slot - 1);
    STAMP(7 + 4 * t);
    {
      const unsigned ka = kbase + slot * 16384;
#pragma unroll
      for (int kb = 0; kb < 4; ++kb) {
        S[0][kb] = f32x4{0, 0, 0, 0}; S[1][kb] = f32x4{0, 0, 0, 0};
#pragma unroll
        for (int s = 0; s < 4; ++s) {
          bf16x8 A = lds_rd16(ka + kb * 4096 + s * 256);
          S[0][kb] = mfma16(A, qf[0][s], S[0][kb]);
          S[1][kb] = mfma16(A, qf[1][s], S[1][kb]);
        }
      }
      float mx[3];
      mx[2] = -1e30f;
      if (act9) {
        const unsigned k9 = ka + (wid & 1) * 8192;
#pragma unroll
        for (int kb = 0; kb < 2; ++kb) {
          S9[kb] = f32x4{0, 0, 0, 0};
#pragma unroll
          for (int s = 0; s < 4; ++s) S9[kb] = mfma16(lds_rd16(k9 + kb * 4096 + s * 256), qf[2][s], S9[kb]);
        }
        float m = S9[0][0];
#pragma unroll
        for (int kb = 0; kb < 2; ++kb)
#pragma unroll
          for (int r = 0; r < 4; ++r) m = fmaxf(m, S9[kb][r]);
        mx[2] = xmax_groups(m);
      }
#pragma unroll
      for (int qb = 0; qb < 2; ++qb) {
        float m = S[qb][0][0];
#pragma unroll
        for (int kb = 0; kb < 4; ++kb)
#pragma unroll
          for (int r = 0; r < 4; ++r) m = fmaxf(m, S[qb][kb][r]);
        mx[qb] = xmax_groups(m);
      }
      const bool need = (mx[0] - mr[0] > THR) || (mx[1] - mr[1] > THR) || (mx[2] - mr[2] > THR);
      if (__any(need)) {
#pragma unroll
        for (int qb = 0; qb < 3; ++qb) {
          const float mn = fmaxf(mr[qb], mx[qb]);
          const float al = __builtin_amdgcn_exp2f(mr[qb] - mn);
          mr[qb] = mn; ls[qb] *= al;
#pragma unroll
          for (int d = 0; d < 8; ++d) { if (qb < 2) O[qb][d] *= al; else O9[d] *= al; }
        }
      }
#pragma unroll
      for (int qb = 0; qb < 2; ++qb) {
        float sum = 0.f;
#pragma unroll
        for (int kb = 0; kb < 4; ++kb)
#pragma unroll
          for (int r = 0; r < 4; ++r) { const float p = __builtin_amdgcn_exp2f(S[qb][kb][r] - mr[qb]); S[qb][kb][r] = p; sum += p; }
        ls[qb] += sum;
#pragma unroll
        for (int jj = 0; jj < 2; ++jj) {
          u32x4 w = {cvtpk(S[qb][2 * jj][0], S[qb][2 * jj][1]), cvtpk(S[qb][2 * jj][2], S[qb][2 * jj][3]),
                     cvtpk(S[qb][2 * jj + 1][0], S[qb][2 * jj + 1][1]), cvtpk(S[qb][2 * jj + 1][2], S[qb][2 * jj + 1][3])};
          P[qb][jj] = __builtin_bit_cast(bf16x8, w);
        }
      }
      if (act9) {
        float sum = 0.f;
#pragma unroll
        for (int kb = 0; kb < 2; ++kb)
#pragma unroll
          for (int r = 0; r < 4; ++r) { const float p = __builtin_amdgcn_exp2f(S9[kb][r] - mr[2]); S9[kb][r] = p; sum += p; }
        ls[2] += sum;
        u32x4 w = {cvtpk(S9[0][0], S9[0][1]), cvtpk(S9[0][2], S9[0][3]), cvtpk(S9[1][0], S9[1][1]), cvtpk(S9[1][2], S9[1][3])};
        P9 = __builtin_bit_cast(bf16x8, w);
      }
    }
    slot = slot == 4 ? 0 : slot + 1;
    STAMP(8 + 4 * t);
    if (t < 6) waitv<12>(); else if (t == 6) waitv<8>(); else waitv<0>();
    BARRIER();
    if (t < 6) load_item(2 * t + 5, slot == 0 ? 4 : slot - 1);
    STAMP(9 + 4 * t);
    {
      const unsigned va0 = vbase0 + slot * 16384, va1 = vbase1 + slot * 16384;
#pragma unroll
      for (int jj = 0; jj < 2; ++jj) {
#pragma unroll
        for (int d = 0; d < 8; ++d) {
          const unsigned a0 = ((d & 1) ? va1 : va0) + (d & ~1) * 128 + jj * 8192;
          bf16x8 A = lds_tr2(a0, a0 + 4096);
          O[0][d] = mfma16(A, P[0][jj], O[0][d]);
          O[1][d] = mfma16(A, P[1][jj], O[1][d]);
        }
      }
      if (act9) {
        const unsigned w9 = (wid & 1) * 8192;
#pragma unroll
        for (int d = 0; d < 8; ++d) {
          const unsigned a0 = ((d & 1) ? va1 : va0) + (d & ~1) * 128 + w9;
          O9[d] = mfma16(lds_tr2(a0, a0 + 4096), P9, O9[d]);
        }
      }
    }
    slot = slot == 4 ? 0 : slot + 1;
    STAMP(10 + 4 * t);
  }
  BARRIER();

  {
  int tz = threadIdx.x; asm volatile("" : "+v"(tz));
  const int tid = tz, lane = tz & 63, l15 = lane & 15, g = lane >> 4;
  ls[0] = xsum_groups(ls[0]); ls[1] = xsum_groups(ls[1]); ls[2] = xsum_groups(ls[2]);
#pragma unroll
  for (int d = 0; d < 8; ++d) *lp<lds_f32x4>(lb + (unsigned)((wid * 8 + d) * 1024) + lane * 16) = O9[d];
  if (g == 0) {
    *lp<lds_f32>(lb + 32768 + wid * 64 + l15 * 4) = mr[2];
    *lp<lds_f32>(lb + 32768 + 256 + wid * 64 + l15 * 4) = ls[2];
  }
  const unsigned L_O = lb + 36864;
#pragma unroll
  for (int qb = 0; qb < 2; ++qb) {
    const float il = 1.0f / ls[qb];
    const unsigned row = (unsigned)((2 * wid + qb) * 16 + l15);
#pragma unroll
    for (int d = 0; d < 8; ++d) {
      u32x2 w = {cvtpk(O[qb][d][0] * il, O[qb][d][1] * il), cvtpk(O[qb][d][2] * il, O[qb][d][3] * il)};
      *lp<lds_u32x2>(L_O + 256u * row + 16u * ((unsigned)(2 * d + (g >> 1)) ^ l15) + 8u * (g & 1)) = w;
    }
  }
  BARRIER();
  {
    float mw[4], lw[4], M = -1e30f;
#pragma unroll
    for (int w = 0; w < 4; ++w) { mw[w] = *lp<lds_f32>(lb + 32768 + w * 64 + l15 * 4); lw[w] = *lp<lds_f32>(lb + 32768 + 256 + w * 64 + l15 * 4); M = fmaxf(M, mw[w]); }
    float L = 0.f, sc[4];
#pragma unroll
    for (int w = 0; w < 4; ++w) { sc[w] = __builtin_amdgcn_exp2f(mw[w] - M); L += lw[w] * sc[w]; }
    const float il = 1.0f / L;
    const unsigned row = 128u + l15;
#pragma unroll
    for (int j = 0; j < 2; ++j) {
      const int d = 2 * wid + j;
      f32x4 a = {0, 0, 0, 0};
#pragma unroll
      for (int w = 0; w < 4; ++w) a += *lp<lds_f32x4>(lb + (unsigned)((w * 8 + d) * 1024) + lane * 16) * sc[w];
      u32x2 w2 = {cvtpk(a[0] * il, a[1] * il), cvtpk(a[2] * il, a[3] * il)};
      *lp<lds_u32x2>(L_O + 256u * row + 16u * ((unsigned)(2 * d + (g >> 1)) ^ l15) + 8u * (g & 1)) = w2;
    }
  }
  BARRIER();
  {
    u16* og = o_ws + ((size_t)(br * 64 + 2 * h) * MROWS + (size_t)b * SQ) * 64;
#pragma unroll
    for (int i = 0; i < 9; ++i) {
      const unsigned row = (unsigned)(i * 16 + (tid >> 4)), c = tid & 15;
      u32x4 v = *lp<lds_u32x4>(L_O + 256u * row + 16u * (c ^ (row & 15)));
      *reinterpret_cast<u32x4*>(og + (size_t)(c >> 3) * MROWS * 64 + (size_t)row * 64 + (c & 7) * 8) = v;
    }
  }
  }
  STAMP(40);
}

constexpr int K2_STAGE = 40960, K2_LDS = K2_STAGE * 4;

__global__ __launch_bounds__(256, 1)
void k_oproj(const u16* __restrict__ o_ws, const u16* __restrict__ woA, const u16* __restrict__ woB, u16* __restrict__ out, unsigned long long* __restrict__ dbg) {
  extern __shared__ __attribute__((aligned(16))) char smem[];
  const unsigned lb = (unsigned)(uintptr_t)smem;
  const int tid = threadIdx.x, lane = tid & 63;
  const int wid = __builtin_amdgcn_readfirstlane(tid >> 6);
  const int bid = blockIdx.x;
  const int xcd = bid & 7, idx = bid >> 3;
  const int br = xcd >> 2, mt = (xcd & 3) * 3 + idx / 10, nt = idx % 10;
  const int nh = wid & 1, kh = wid >> 1;
  const rsrc_t rO = mkrsrc(o_ws + ((size_t)br * 64 * MROWS + (size_t)mt * 96) * 64, (unsigned)((64 * MROWS - mt * 96) * 128));
  const rsrc_t rW = mkrsrc((br ? woB : woA) + nt * 64, (unsigned)(HD * DM * 2 - nt * 128));
  const int l15 = lane & 15, g = lane >> 4, q4 = l15 >> 2, p4 = lane & 3;

  const unsigned ovo = (unsigned)((lane & 7) * 128 + (lane >> 3) * 16);
  const unsigned wri = (unsigned)(l15 >> 1);
  const unsigned wvo = (8u * (wri >> 2) + (wri & 3)) * (DM * 2) + g * 32 + (lane & 1) * 16;
  auto load_stage = [&](int c) {
    const unsigned sb = lb + (c & 3) * K2_STAGE;
    dma3(rO, sb + wid * 3072, ovo, (unsigned)(2 * c) * (MROWS * 128) + wid * 3072);
    dma3(rO, sb + 12288 + wid * 3072, ovo, (unsigned)(2 * c + 1) * (MROWS * 128) + wid * 3072);
#pragma unroll
    for (int i = 0; i < 4; ++i) {
      const unsigned pc = wid * 4 + i;
      dma1(rW, sb + 24576 + pc * 1024, wvo, (unsigned)(c * 128 + 16 * (pc >> 1) + 4 * (pc & 1)) * (DM * 2));
    }
  };
  f32x4 acc[2][6];
#pragma unroll
  for (int n = 0; n < 2; ++n)
#pragma unroll
    for (int m = 0; m < 6; ++m) acc[n][m] = f32x4{0, 0, 0, 0};
  const unsigned boff = (unsigned)((l15 >> 3) * 1024 + (4 * kh + g) * 128 + (l15 & 7) * 16);
  const unsigned aoff = 24576u + (unsigned)(2 * nh) * 256 + kh * 4096 + (g >> 1) * 2048 + (q4 + 4 * (g & 1)) * 32 + 8 * p4;

  STAMP(0);
  load_stage(0); load_stage(1); load_stage(2);
  for (int c = 0; c < 32; ++c) {
    if (c < 30) waitv<20>(); else if (c < 31) waitv<10>(); else waitv<0>();
    BARRIER();
    if (c + 3 < 32) load_stage(c + 3);
    if (c < 16) STAMP(1 + c);
    const unsigned sb = lb + (c & 3) * K2_STAGE;
#pragma unroll
    for (int sc = 0; sc < 2; ++sc) {
      bf16x8 Bf[6];
#pragma unroll
      for (int m = 0; m < 6; ++m) Bf[m] = lds_rd16(sb + sc * 12288 + boff + m * 2048);
#pragma unroll
      for (int n = 0; n < 2; ++n) {
        const unsigned a0 = sb + aoff + n * 256 + sc * 8192;
        bf16x8 A = lds_tr2(a0, a0 + 1024);
#pragma unroll
        for (int m = 0; m < 6; ++m) acc[n][m] = mfma16(A, Bf[m], acc[n][m]);
      }
    }
  }
  STAMP(33);
  BARRIER();
  if (kh == 1) {
#pragma unroll
    for (int n = 0; n < 2; ++n)
#pragma unroll
      for (int m = 0; m < 6; ++m) *lp<lds_f32x4>(lb + (unsigned)(((nh * 12) + n * 6 + m) * 1024) + lane * 16) = acc[n][m];
  }
  BARRIER();
  if (kh == 0) {
    u16* og = out + (size_t)(mt * 96) * (2 * DM) + br * DM + nt * 64;
#pragma unroll
    for (int n = 0; n < 2; ++n)
#pragma unroll
      for (int m = 0; m < 6; ++m) {
        f32x4 v = acc[n][m] + *lp<lds_f32x4>(lb + (unsigned)(((nh * 12) + n * 6 + m) * 1024) + lane * 16);
        u32x2 w = {cvtpk(v[0], v[1]), cvtpk(v[2], v[3])};
        *reinterpret_cast<u32x2*>(og + (size_t)(m * 16 + l15) * (2 * DM) + (2 * nh + n) * 16 + 4 * g) = w;
      }
  }
  STAMP(34);
}

extern "C" void kernel_launch(void* const* d_in, const int* in_sizes, int n_in,
                              void* d_out, int out_size, void* d_ws, size_t ws_size,
                              hipStream_t stream) {
  const u16* xA = (const u16*)d_in[0];
  const u16* kA = (const u16*)d_in[1];
  const u16* vA = (const u16*)d_in[2];
  const u16* wqA = (const u16*)d_in[3];
  const u16* woA = (const u16*)d_in[4];
  const u16* xB = (const u16*)d_in[5];
  const u16* kB = (const u16*)d_in[6];
  const u16* vB = (const u16*)d_in[7];
  const u16* wqB = (const u16*)d_in[8];
  const u16* woB = (const u16*)d_in[9];
  u16* o_ws = (u16*)d_ws;
  unsigned long long* dbg = (unsigned long long*)((char*)d_ws + (200u << 20));
  (void)hipFuncSetAttribute((const void*)k_attn, hipFuncAttributeMaxDynamicSharedMemorySize, K1_LDS);
  (void)hipFuncSetAttribute((const void*)k_oproj, hipFuncAttributeMaxDynamicSharedMemorySize, K2_LDS);
  k_attn<<<512, 256, K1_LDS, stream>>>(xA, kA, vA, wqA, xB, kB, vB, wqB, o_ws, dbg);
  k_oproj<<<240, 256, K2_LDS, stream>>>(o_ws, woA, woB, (u16*)d_out, dbg + 512 * 64);
}
```

```cpp
#include <hip/hip_runtime.h>
#include <hip/hip_bf16.h>
#include <stdint.h>
#include <type_traits>

typedef __attribute__((ext_vector_type(8))) short bf16x8;
typedef __attribute__((ext_vector_type(4))) short s16x4;
typedef __attribute__((ext_vector_type(4))) float f32x4;
typedef __attribute__((ext_vector_type(2))) float f32x2;
typedef __attribute__((ext_vector_type(2))) __bf16 bf16x2_t;
typedef __attribute__((ext_vector_type(4))) unsigned u32x4;
typedef __attribute__((ext_vector_type(2))) unsigned u32x2;
typedef unsigned short u16;
typedef __attribute__((address_space(3))) bf16x8 lds_bf16x8;
typedef __attribute__((address_space(3))) s16x4 lds_s16x4;
typedef __attribute__((address_space(3))) f32x4 lds_f32x4;
typedef __attribute__((address_space(3))) u32x4 lds_u32x4;
typedef __attribute__((address_space(3))) u32x2 lds_u32x2;
typedef __attribute__((address_space(3))) float lds_f32;
typedef __amdgpu_buffer_rsrc_t rsrc_t;

#define K1GRID 512
#define DI __device__ __forceinline__
#define NTW ""
#define NTK2 ""
template <class T> DI T* lp(unsigned a) { return (T*)(uintptr_t)a; }

constexpr int NB = 8, SQ = 144, DM = 640, NH = 32, SK = 512, DH = 128;
constexpr int HD = NH * DH;
constexpr int MROWS = NB * SQ;
constexpr float QSCALE = 0.088388347648318440f * 1.4426950408889634f;
constexpr float THR = 8.0f;

DI unsigned cvtpk(float lo, float hi) {
  f32x2 v = {lo, hi};
  bf16x2_t b = __builtin_convertvector(v, bf16x2_t);
  return __builtin_bit_cast(unsigned, b);
}
DI f32x4 mfma16(bf16x8 a, bf16x8 b, f32x4 c) { return __builtin_amdgcn_mfma_f32_16x16x32_bf16(a, b, c, 0, 0, 0); }
DI rsrc_t mkrsrc(const void* p, unsigned bytes) { return __builtin_amdgcn_make_buffer_rsrc((void*)p, (short)0, (int)bytes, 0x00020000); }
DI void dma1(rsrc_t r, unsigned lds_addr, unsigned voff, unsigned soff) {
  asm volatile("s_mov_b32 m0, %0\n\ts_nop 0\n\tbuffer_load_dwordx4 %1, %2, %3 offen" NTK2 " lds" :: "s"(lds_addr), "v"(voff), "s"(r), "s"(soff) : "memory", "m0");
}
DI void dma1w(rsrc_t r, unsigned lds_addr, unsigned voff, unsigned soff) {
  asm volatile("s_mov_b32 m0, %0\n\ts_nop 0\n\tbuffer_load_dwordx4 %1, %2, %3 offen lds" :: "s"(lds_addr), "v"(voff), "s"(r), "s"(soff) : "memory", "m0");
}
DI void dma4(rsrc_t r, unsigned lds_addr, unsigned v0, unsigned v1, unsigned v2, unsigned v3, unsigned soff) {
  asm volatile("s_mov_b32 m0, %0\n\ts_nop 0\n\t"
               "buffer_load_dwordx4 %1, %5, %6 offen nt lds\n\t"
               "buffer_load_dwordx4 %2, %5, %6 offen offset:1024 nt lds\n\t"
               "buffer_load_dwordx4 %3, %5, %6 offen offset:2048 nt lds\n\t"
               "buffer_load_dwordx4 %4, %5, %6 offen offset:3072 nt lds"
               :: "s"(lds_addr), "v"(v0), "v"(v1), "v"(v2), "v"(v3), "s"(r), "s"(soff) : "memory", "m0");
}
DI void dma3(rsrc_t r, unsigned lds_addr, unsigned v0, unsigned v1, unsigned soff) {
  asm volatile("s_mov_b32 m0, %0\n\ts_nop 0\n\t"
               "buffer_load_dwordx4 %1, %3, %4 offen" NTK2 " lds\n\t"
               "buffer_load_dwordx4 %2, %3, %4 offen offset:1024" NTK2 " lds\n\t"
               "buffer_load_dwordx4 %1, %3, %4 offen offset:2048" NTK2 " lds"
               :: "s"(lds_addr), "v"(v0), "v"(v1), "s"(r), "s"(soff) : "memory", "m0");
}
DI bf16x8 bload_asm(rsrc_t r, unsigned voff, unsigned soff) {
  bf16x8 v;
  asm volatile("buffer_load_dwordx4 %0, %1, %2, %3 offen" NTW : "=v"(v) : "v"(voff), "s"(r), "s"(soff) : "memory");
  return v;
}
DI bf16x8 lds_rd16(unsigned addr) { return *lp<lds_bf16x8>(addr); }
DI bf16x8 lds_tr2(unsigned a0, unsigned a1) {
  s16x4 lo = __builtin_amdgcn_ds_read_tr16_b64_v4i16(lp<lds_s16x4>(a0));
  s16x4 hi = __builtin_amdgcn_ds_read_tr16_b64_v4i16(lp<lds_s16x4>(a1));
  return __builtin_shufflevector(lo, hi, 0, 1, 2, 3, 4, 5, 6, 7);
}
DI float xmax_groups(float x) {
  auto a = __builtin_amdgcn_permlane16_swap(__float_as_uint(x), __float_as_uint(x), false, false);
  x = fmaxf(__uint_as_float(a[0]), __uint_as_float(a[1]));
  auto b = __builtin_amdgcn_permlane32_swap(__float_as_uint(x), __float_as_uint(x), false, false);
  return fmaxf(__uint_as_float(b[0]), __uint_as_float(b[1]));
}
DI float xsum_groups(float x) {
  auto a = __builtin_amdgcn_permlane16_swap(__float_as_uint(x), __float_as_uint(x), false, false);
  x = __uint_as_float(a[0]) + __uint_as_float(a[1]);
  auto b = __builtin_amdgcn_permlane32_swap(__float_as_uint(x), __float_as_uint(x), false, false);
  return __uint_as_float(b[0]) + __uint_as_float(b[1]);
}
#define STAMP(i)
template <int N> DI void waitv() { asm volatile("s_waitcnt vmcnt(%0)" :: "i"(N) : "memory"); }
#define WAITL0() asm volatile("s_waitcnt lgkmcnt(0)" ::: "memory")
#define BARRIER() asm volatile("s_waitcnt lgkmcnt(0)\n\ts_barrier" ::: "memory")
#define SBAR() __builtin_amdgcn_sched_barrier(0)

constexpr int K1_LDS = 81920;
#define PFD 6
#define IC(x) std::integral_constant<int, x>{}

__global__ __launch_bounds__(256, 2)
void k_attn(const u16* __restrict__ xA, const u16* __restrict__ kA, const u16* __restrict__ vA, const u16* __restrict__ wqA,
            const u16* __restrict__ xB, const u16* __restrict__ kB, const u16* __restrict__ vB, const u16* __restrict__ wqB,
            const u16* __restrict__ woA, const u16* __restrict__ woB,
            u16* __restrict__ o_ws, unsigned long long* __restrict__ dbg) {
  extern __shared__ __attribute__((aligned(16))) char smem[];
  const unsigned lb = (unsigned)(uintptr_t)smem;
  const int bid = blockIdx.x;
  const int xcd = bid & 7, idx = bid >> 3;
  const int h = (xcd & 3) * 8 + (idx & 7), b = idx >> 3, br = xcd >> 2;
  const int tid = threadIdx.x, lane = tid & 63;
  const int wid = __builtin_amdgcn_readfirstlane(tid >> 6);
  const int l15 = lane & 15, g = lane >> 4, q4 = l15 >> 2, p4 = lane & 3;
  const unsigned L_W = lb + 4 * 16384, L_XS = lb + 2 * 16384;
#define LT(i)
  LT(0);

  if (wid == 3) {
    const rsrc_t rX = mkrsrc((br ? xB : xA) + (size_t)b * SQ * DM, SQ * DM * 2);
    const rsrc_t rW = mkrsrc((br ? wqB : wqA) + h * DH, (unsigned)(DM * HD * 2 - h * DH * 2));
    const rsrc_t rK = mkrsrc((br ? kB : kA) + (size_t)(b * NH + h) * SK * DH, SK * DH * 2);
    const rsrc_t rV = mkrsrc((br ? vB : vA) + (size_t)(b * NH + h) * SK * DH, SK * DH * 2);
    unsigned kvo[4], vvo[2];
#pragma unroll
    for (int p = 0; p < 4; ++p) kvo[p] = (unsigned)(g * 256 + ((l15 ^ (4 * p + g)) * 16));
#pragma unroll
    for (int p = 0; p < 2; ++p) vvo[p] = (unsigned)(g * 256 + ((l15 ^ ((4 * p + g) << 1)) * 16));
    auto load_item = [&](int n, int slot) {
      const unsigned la = lb + slot * 16384, so = (unsigned)(n >> 1) * 16384;
#pragma unroll
      for (int q = 0; q < 4; ++q) {
        if (n & 1) dma4(rV, la + q * 4096, vvo[0], vvo[1], vvo[0], vvo[1], so + q * 4096);
        else       dma4(rK, la + q * 4096, kvo[0], kvo[1], kvo[2], kvo[3], so + q * 4096);
      }
    };
    const unsigned xv = (unsigned)((lane >> 2) * (DM * 2) + (lane & 3) * 16);
    const unsigned wv = (unsigned)(g * (HD * 2) + l15 * 16);
    unsigned xw = L_XS + (lane >> 2) * 64 + (((lane & 3) ^ ((0x1320u >> (4 * ((lane >> 4) & 3))) & 3)) * 16);
    unsigned ww0 = L_W + g * 256 + ((l15 ^ (g << 1)) * 16);
    unsigned ww1 = L_W + g * 256 + ((l15 ^ ((g | 4) << 1)) * 16);
    struct { bf16x8 sf[3][17]; } R;
    int cn = 0;
    auto sload = [&](auto B) { constexpr int bb = B;
#pragma unroll
      for (int i = 0; i < 9; ++i) R.sf[bb][i] = bload_asm(rX, xv, (unsigned)cn * 64 + i * 16 * DM * 2);
#pragma unroll
      for (int i = 0; i < 8; ++i) R.sf[bb][9 + i] = bload_asm(rW, wv, (unsigned)(cn * 32 + 4 * i) * (HD * 2));
      ++cn; };
    auto swait = [&](auto B, auto N) { constexpr int bb = B; auto& r = R.sf[bb];
      asm volatile("s_waitcnt vmcnt(%17)" : "+v"(r[0]), "+v"(r[1]), "+v"(r[2]), "+v"(r[3]), "+v"(r[4]), "+v"(r[5]), "+v"(r[6]), "+v"(r[7]), "+v"(r[8]),
                   "+v"(r[9]), "+v"(r[10]), "+v"(r[11]), "+v"(r[12]), "+v"(r[13]), "+v"(r[14]), "+v"(r[15]), "+v"(r[16]) : "i"((int)N) : "memory"); };
    auto swrite = [&](auto B) { constexpr int bb = B;
#pragma unroll
      for (int i = 0; i < 9; ++i) *lp<lds_bf16x8>(xw + i * 1024) = R.sf[bb][i];
#pragma unroll
      for (int i = 0; i < 8; ++i) *lp<lds_bf16x8>((((i >> 1) & 1) ? ww1 : ww0) + i * 1024) = R.sf[bb][9 + i];
      xw ^= 16384; ww0 ^= 8192; ww1 ^= 8192;
      __builtin_amdgcn_sched_barrier(0); };
    sload(IC(0)); sload(IC(1)); sload(IC(2));
    swait(IC(0), IC(34)); swrite(IC(0)); sload(IC(0));
    swait(IC(1), IC(34)); swrite(IC(1)); sload(IC(1));
    BARRIER();
    auto lstep = [&](auto I) { constexpr int i = I, bw = (i + 2) % 3;
      constexpr int last = (i + 4 < 20) ? (i + 4) : 19;
      if constexpr (i + 2 < 20) swait(IC(bw), IC((last - (i + 2)) * 17));
      BARRIER();
      if constexpr (i + 2 < 20) swrite(IC(bw));
      if constexpr (i + 5 < 20) sload(IC(bw));
      if constexpr (i == 0 || i == 5) load_item(i / 5, i / 5);
    };
#define LS(i) lstep(IC(i))
    LS(0); LS(1); LS(2); LS(3); LS(4); LS(5); LS(6); LS(7); LS(8); LS(9);
    LS(10); LS(11); LS(12); LS(13); LS(14); LS(15); LS(16); LS(17); LS(18); LS(19);
#undef LS
    BARRIER();
    load_item(2, 2);
    load_item(3, 3);
    BARRIER();
    load_item(4, 4);
    int slot = 0;
    for (int n = 0; n < 12; ++n) {
      if (n == 0) waitv<63>(); else waitv<48>();
      BARRIER();
      if (n >= 1) load_item(n + 4, slot == 0 ? 4 : slot - 1);
      slot = slot == 4 ? 0 : slot + 1;
    }
    {
      const rsrc_t rWo = mkrsrc((bid < 256 ? woA : woB) + (size_t)(bid & 255) * 10240, 20480);
      bf16x8 dm[20];
#pragma unroll
      for (int i = 0; i < 20; ++i) dm[i] = bload_asm(rWo, lane * 16, (unsigned)i * 1024);
      waitv<63>(); BARRIER();
      waitv<52>(); BARRIER();
      waitv<36>(); BARRIER();
      waitv<20>(); BARRIER();
      waitv<0>();
#pragma unroll
      for (int i = 0; i < 20; ++i) asm volatile("" :: "v"(dm[i]));
    }
    BARRIER();
    BARRIER();
  } else {
    {
      const rsrc_t rWa = mkrsrc((br ? wqB : wqA) + (xcd & 3) * 8 * DH, (unsigned)(DM * HD * 2 - (xcd & 3) * 8 * DH * 2));
      const rsrc_t rXa = mkrsrc((br ? xB : xA), NB * SQ * DM * 2);
      const unsigned dump = lb + 59392 + wid * 1024, lv = lane * 16;
#pragma unroll
      for (int i = 0; i < 7; ++i) { const unsigned p = wid * 7 + i; if (p < 20) dma1w(rWa, dump, lv, (unsigned)(idx * 10 + (p >> 1)) * (HD * 2) + (p & 1) * 1024); }
#pragma unroll
      for (int i = 0; i < 8; ++i) { const unsigned p = wid * 8 + i; if (p < 23) dma1w(rXa, dump, lv, (unsigned)idx * 23040 + p * 1024); }
    }
    f32x4 qa[8][3];
#pragma unroll
    for (int j = 0; j < 8; ++j) { qa[j][0] = f32x4{0, 0, 0, 0}; qa[j][1] = f32x4{0, 0, 0, 0}; qa[j][2] = f32x4{0, 0, 0, 0}; }
    {
      unsigned xr = L_XS + (48 * wid + l15) * 64 + ((g ^ ((0x1320u >> (4 * (l15 >> 2))) & 3)) * 16);
      unsigned wb = L_W + (unsigned)((8 * g + q4) * 256 + 8 * (p4 & 1) + 16 * (p4 >> 1)) + 32u * (unsigned)(q4 | ((g & 1) << 2));
      bf16x8 fx[2][3], fa[2][8];
      auto fread = [&](auto P) { constexpr int p = P;
        fx[p][0] = lds_rd16(xr); fx[p][1] = lds_rd16(xr + 1024); fx[p][2] = lds_rd16(xr + 2048);
#pragma unroll
        for (int j = 0; j < 8; ++j) { const unsigned a0 = wb ^ (32u * j); fa[p][j] = lds_tr2(a0, a0 + 1024); }
        xr ^= 16384; wb ^= 8192; };
      BARRIER();
      fread(IC(0));
      LT(1);
      auto cstep = [&](auto P, bool rd) { constexpr int p = P;
        BARRIER();
        if (rd) fread(IC(p ^ 1));
#pragma unroll
        for (int j = 0; j < 8; ++j) {
          qa[j][0] = mfma16(fa[p][j], fx[p][0], qa[j][0]);
          qa[j][1] = mfma16(fa[p][j], fx[p][1], qa[j][1]);
          qa[j][2] = mfma16(fa[p][j], fx[p][2], qa[j][2]);
        } };
      for (int it = 0; it < 10; ++it) { cstep(IC(0), true); cstep(IC(1), it < 9); }
    }
    LT(2);
    waitv<0>();
    BARRIER();
    bf16x8 qf[3][4];
    {
      const unsigned wreg = L_W + wid * 4096;
      const unsigned wq_l = wreg + (unsigned)((l15 >> 3) * 2048 + (l15 & 7) * 16 + (g >> 1) * 128 + (g & 1) * 8);
      const unsigned rq_l = wreg + (unsigned)((l15 >> 3) * 2048 + (l15 & 7) * 16 + g * 128);
#pragma unroll
      for (int qb = 0; qb < 3; ++qb) {
#pragma unroll
        for (int j = 0; j < 8; ++j) {
          u32x2 w = {cvtpk(qa[j][qb][0] * QSCALE, qa[j][qb][1] * QSCALE), cvtpk(qa[j][qb][2] * QSCALE, qa[j][qb][3] * QSCALE)};
          *lp<lds_u32x2>(wq_l + j * 256) = w;
        }
        WAITL0();
#pragma unroll
        for (int s = 0; s < 4; ++s) qf[qb][s] = lds_rd16(rq_l + s * 512);
        WAITL0();
      }
    }
    BARRIER();
    LT(3);
    f32x4 O[3][8];
#pragma unroll
    for (int d = 0; d < 8; ++d) { O[0][d] = f32x4{0, 0, 0, 0}; O[1][d] = f32x4{0, 0, 0, 0}; O[2][d] = f32x4{0, 0, 0, 0}; }
    float mr[3] = {-1e30f, -1e30f, -1e30f}, ls[3] = {0.f, 0.f, 0.f};
    unsigned koff[4];
#pragma unroll
    for (int s = 0; s < 4; ++s) koff[s] = lb + 256u * l15 + 16u * ((unsigned)(4 * s + g) ^ (unsigned)l15);
    const unsigned vbase = lb + 256u * (4 * g + q4) + 8u * (p4 & 1) + 16u * (p4 >> 1) + 32u * (unsigned)(4 * (g & 1) + q4);
    f32x4 S[3][4];
    bf16x8 P[3][2];
    int slot = 0;
    for (int t = 0; t < 8; ++t) {
      BARRIER();
      {
        const unsigned ka = slot * 16384;
        bf16x8 F[PFD];
#pragma unroll
        for (int f = 0; f < PFD; ++f) F[f] = lds_rd16(koff[f & 3] + ka + (f >> 2) * 4096);
#pragma unroll
        for (int f = 0; f < 16; ++f) {
          const int kb = f >> 2, s = f & 3;
          const bf16x8 A = F[f % PFD];
          if (f + PFD < 16) F[f % PFD] = lds_rd16(koff[(f + PFD) & 3] + ka + ((f + PFD) >> 2) * 4096);
#pragma unroll
          for (int qb = 0; qb < 3; ++qb) S[qb][kb] = mfma16(A, qf[qb][s], s == 0 ? f32x4{0, 0, 0, 0} : S[qb][kb]);
          __builtin_amdgcn_sched_barrier(0);
        }
        float mx[3];
#pragma unroll
        for (int qb = 0; qb < 3; ++qb) {
          float m = S[qb][0][0];
#pragma unroll
          for (int kb = 0; kb < 4; ++kb)
#pragma unroll
            for (int r = 0; r < 4; ++r) m = fmaxf(m, S[qb][kb][r]);
          mx[qb] = xmax_groups(m);
        }
        const bool need = (mx[0] - mr[0] > THR) || (mx[1] - mr[1] > THR) || (mx[2] - mr[2] > THR);
        if (__any(need)) {
#pragma unroll
          for (int qb = 0; qb < 3; ++qb) {
            const float mn = fmaxf(mr[qb], mx[qb]);
            const float al = __builtin_amdgcn_exp2f(mr[qb] - mn);
            mr[qb] = mn; ls[qb] *= al;
#pragma unroll
            for (int d = 0; d < 8; ++d) O[qb][d] *= al;
          }
        }
#pragma unroll
        for (int qb = 0; qb < 3; ++qb) {
          float sum = 0.f;
#pragma unroll
          for (int kb = 0; kb < 4; ++kb)
#pragma unroll
            for (int r = 0; r < 4; ++r) { const float p = __builtin_amdgcn_exp2f(S[qb][kb][r] - mr[qb]); S[qb][kb][r] = p; sum += p; }
          ls[qb] += sum;
#pragma unroll
          for (int jj = 0; jj < 2; ++jj) {
            u32x4 w = {cvtpk(S[qb][2 * jj][0], S[qb][2 * jj][1]), cvtpk(S[qb][2 * jj][2], S[qb][2 * jj][3]),
                       cvtpk(S[qb][2 * jj + 1][0], S[qb][2 * jj + 1][1]), cvtpk(S[qb][2 * jj + 1][2], S[qb][2 * jj + 1][3])};
            P[qb][jj] = __builtin_bit_cast(bf16x8, w);
          }
        }
      }
      slot = slot == 4 ? 0 : slot + 1;
      BARRIER();
      {
        const unsigned va = vbase + slot * 16384;
        bf16x8 F[PFD];
#pragma unroll
        for (int f = 0; f < PFD; ++f) { const unsigned a0 = (va ^ (32u * (f & 7))) + (f >> 3) * 8192; F[f] = lds_tr2(a0, a0 + 4096); }
#pragma unroll
        for (int f = 0; f < 16; ++f) {
          const int jj = f >> 3, d = f & 7;
          const bf16x8 A = F[f % PFD];
          if (f + PFD < 16) { const unsigned a0 = (va ^ (32u * ((f + PFD) & 7))) + ((f + PFD) >> 3) * 8192; F[f % PFD] = lds_tr2(a0, a0 + 4096); }
#pragma unroll
          for (int qb = 0; qb < 3; ++qb) O[qb][d] = mfma16(A, P[qb][jj], O[qb][d]);
          __builtin_amdgcn_sched_barrier(0);
        }
      }
      slot = slot == 4 ? 0 : slot + 1;
    }
    BARRIER();
    LT(4);
    {
      int tz = threadIdx.x; asm volatile("" : "+v"(tz));
      const int lane = tz & 63, l15 = lane & 15, g = lane >> 4;
#pragma unroll
      for (int qb = 0; qb < 3; ++qb) {
        const float il = 1.0f / xsum_groups(ls[qb]);
        const unsigned row = (unsigned)((3 * wid + qb) * 16 + l15);
#pragma unroll
        for (int d = 0; d < 8; ++d) {
          u32x2 w = {cvtpk(O[qb][d][0] * il, O[qb][d][1] * il), cvtpk(O[qb][d][2] * il, O[qb][d][3] * il)};
          *lp<lds_u32x2>(lb + 256u * row + 16u * ((unsigned)(2 * d + (g >> 1)) ^ l15) + 8u * (g & 1)) = w;
        }
      }
    }
    BARRIER();
  }
  {
    int tz = threadIdx.x; asm volatile("" : "+v"(tz));
    u16* og = o_ws + ((size_t)(br * 64 + 2 * h) * MROWS + (size_t)b * SQ) * 64;
#pragma unroll
    for (int i = 0; i < 9; ++i) {
      const unsigned row = (unsigned)(i * 16 + (tz >> 4)), c = tz & 15;
      u32x4 v = *lp<lds_u32x4>(lb + 256u * row + 16u * (c ^ (row & 15)));
      *reinterpret_cast<u32x4*>(og + (size_t)(c >> 3) * MROWS * 64 + (size_t)row * 64 + (c & 7) * 8) = v;
    }
  }
}

constexpr int K2_STAGE = 40960, K2_LDS = K2_STAGE * 4;

__global__ __launch_bounds__(256, 1)
void k_oproj(const u16* __restrict__ o_ws, const u16* __restrict__ woA, const u16* __restrict__ woB, u16* __restrict__ out, unsigned long long* __restrict__ dbg) {
  extern __shared__ __attribute__((aligned(16))) char smem[];
  const unsigned lb = (unsigned)(uintptr_t)smem;
  const int tid = threadIdx.x, lane = tid & 63;
  const int wid = __builtin_amdgcn_readfirstlane(tid >> 6);
  const int bid = blockIdx.x;
  const int xcd = bid & 7, idx = bid >> 3;
  const int br = xcd >> 2, mt = (xcd & 3) * 3 + idx / 10, nt = idx % 10;
  const int nh = wid & 1, kh = wid >> 1;
    const int rot = ((xcd & 3) * 8) & 31;
  const rsrc_t rO = mkrsrc(o_ws + ((size_t)br * 64 * MROWS + (size_t)mt * 96) * 64, (unsigned)((64 * MROWS - mt * 96) * 128));
  const rsrc_t rW = mkrsrc((br ? woB : woA) + nt * 64, (unsigned)(HD * DM * 2 - nt * 128));
  const int l15 = lane & 15, g = lane >> 4, q4 = l15 >> 2, p4 = lane & 3;

  unsigned ovo[2], wvo[2];
#pragma unroll
  for (int p = 0; p < 2; ++p) {
    ovo[p] = (unsigned)((lane >> 3) * 128 + (((lane & 7) ^ (4 * p + (lane >> 4))) * 16));
    wvo[p] = (unsigned)((lane >> 3) * (DM * 2) + (((lane & 7) ^ ((((lane >> 4) & 1) | (p << 1)) << 1)) * 16));
  }
  const unsigned ova = (wid & 1) ? ovo[1] : ovo[0], ovb = (wid & 1) ? ovo[0] : ovo[1];
  auto load_stage = [&](int c0) {
    const unsigned sb = lb + (c0 & 3) * K2_STAGE;
    const int c = (c0 + rot) & 31;
    dma3(rO, sb + wid * 3072, ova, ovb, (unsigned)(2 * c) * (MROWS * 128) + wid * 3072);
    dma3(rO, sb + 12288 + wid * 3072, ova, ovb, (unsigned)(2 * c + 1) * (MROWS * 128) + wid * 3072);
#pragma unroll
    for (int i = 0; i < 4; ++i) {
      const unsigned pc = wid * 4 + i;
      dma1(rW, sb + 24576 + pc * 1024, wvo[i & 1], (unsigned)(c * 128 + 8 * pc) * (DM * 2));
    }
  };
  f32x4 acc[2][6];
#pragma unroll
  for (int n = 0; n < 2; ++n)
#pragma unroll
    for (int m = 0; m < 6; ++m) acc[n][m] = f32x4{0, 0, 0, 0};
  const unsigned boff = (unsigned)(128 * l15 + 16 * ((4 * kh + g) ^ ((l15 >> 1) & 7)));
  unsigned aoff[2];
#pragma unroll
  for (int n = 0; n < 2; ++n) {
    const unsigned fl = (unsigned)(((q4 >> 1) & 1) | ((g & 1) << 1));
    aoff[n] = 24576u + 128u * (32 * kh + 8 * g + q4) + 32u * ((unsigned)(2 * nh + n) ^ fl) + 16u * (p4 >> 1) + 8u * (p4 & 1);
  }

  STAMP(0);
  load_stage(0); load_stage(1); load_stage(2);
  for (int c = 0; c < 32; ++c) {
    if (c < 30) waitv<20>(); else if (c < 31) waitv<10>(); else waitv<0>();
    BARRIER();
    if (c + 3 < 32) load_stage(c + 3);
    if (c < 16) STAMP(1 + c);
    const unsigned sb = lb + (c & 3) * K2_STAGE;
#pragma unroll
    for (int sc = 0; sc < 2; ++sc) {
      bf16x8 Bf[6];
#pragma unroll
      for (int m = 0; m < 6; ++m) Bf[m] = lds_rd16(sb + sc * 12288 + boff + m * 2048);
#pragma unroll
      for (int n = 0; n < 2; ++n) {
        const unsigned a0 = sb + aoff[n] + sc * 8192;
        bf16x8 A = lds_tr2(a0, a0 + 512);
#pragma unroll
        for (int m = 0; m < 6; ++m) acc[n][m] = mfma16(A, Bf[m], acc[n][m]);
      }
    }
  }
  STAMP(33);
  BARRIER();
  if (kh == 1) {
#pragma unroll
    for (int n = 0; n < 2; ++n)
#pragma unroll
      for (int m = 0; m < 6; ++m) *lp<lds_f32x4>(lb + (unsigned)(((nh * 12) + n * 6 + m) * 1024) + lane * 16) = acc[n][m];
  }
  BARRIER();
  if (kh == 0) {
    u16* og = out + (size_t)(mt * 96) * (2 * DM) + br * DM + nt * 64;
#pragma unroll
    for (int n = 0; n < 2; ++n)
#pragma unroll
      for (int m = 0; m < 6; ++m) {
        f32x4 v = acc[n][m] + *lp<lds_f32x4>(lb + (unsigned)(((nh * 12) + n * 6 + m) * 1024) + lane * 16);
        u32x2 w = {cvtpk(v[0], v[1]), cvtpk(v[2], v[3])};
        *reinterpret_cast<u32x2*>(og + (size_t)(m * 16 + l15) * (2 * DM) + (2 * nh + n) * 16 + 4 * g) = w;
      }
  }
  STAMP(34);
}

extern "C" void kernel_launch(void* const* d_in, const int* in_sizes, int n_in,
                              void* d_out, int out_size, void* d_ws, size_t ws_size,
                              hipStream_t stream) {
  const u16* xA = (const u16*)d_in[0];
  const u16* kA = (const u16*)d_in[1];
  const u16* vA = (const u16*)d_in[2];
  const u16* wqA = (const u16*)d_in[3];
  const u16* woA = (const u16*)d_in[4];
  const u16* xB = (const u16*)d_in[5];
  const u16* kB = (const u16*)d_in[6];
  const u16* vB = (const u16*)d_in[7];
  const u16* wqB = (const u16*)d_in[8];
  const u16* woB = (const u16*)d_in[9];
  u16* o_ws = (u16*)d_ws;
  unsigned long long* dbg = (unsigned long long*)((char*)d_ws + (200u << 20));
  (void)hipFuncSetAttribute((const void*)k_attn, hipFuncAttributeMaxDynamicSharedMemorySize, K1_LDS);
  (void)hipFuncSetAttribute((const void*)k_oproj, hipFuncAttributeMaxDynamicSharedMemorySize, K2_LDS);
  k_attn<<<K1GRID, 256, K1_LDS, stream>>>(xA, kA, vA, wqA, xB, kB, vB, wqB, woA, woB, o_ws, dbg);
  k_oproj<<<240, 256, K2_LDS, stream>>>(o_ws, woA, woB, (u16*)d_out, dbg + 512 * 64);
}
```

```cpp
#include <hip/hip_runtime.h>
#include <hip/hip_bf16.h>
#include <stdint.h>
#include <type_traits>

typedef __attribute__((ext_vector_type(8))) short bf16x8;
typedef __attribute__((ext_vector_type(4))) short s16x4;
typedef __attribute__((ext_vector_type(4))) float f32x4;
typedef __attribute__((ext_vector_type(2))) float f32x2;
typedef __attribute__((ext_vector_type(2))) __bf16 bf16x2_t;
typedef __attribute__((ext_vector_type(4))) unsigned u32x4;
typedef __attribute__((ext_vector_type(2))) unsigned u32x2;
typedef unsigned short u16;
typedef __attribute__((address_space(3))) bf16x8 lds_bf16x8;
typedef __attribute__((address_space(3))) s16x4 lds_s16x4;
typedef __attribute__((address_space(3))) f32x4 lds_f32x4;
typedef __attribute__((address_space(3))) u32x4 lds_u32x4;
typedef __attribute__((address_space(3))) u32x2 lds_u32x2;
typedef __attribute__((address_space(3))) float lds_f32;
typedef __amdgpu_buffer_rsrc_t rsrc_t;

#define K1GRID 512
#define DI __device__ __forceinline__
#define NTW ""
#define NTK2 ""
template <class T> DI T* lp(unsigned a) { return (T*)(uintptr_t)a; }

constexpr int NB = 8, SQ = 144, DM = 640, NH = 32, SK = 512, DH = 128;
constexpr int HD = NH * DH;
constexpr int MROWS = NB * SQ;
constexpr float QSCALE = 0.088388347648318440f * 1.4426950408889634f;
constexpr float THR = 8.0f;

DI unsigned cvtpk(float lo, float hi) {
  f32x2 v = {lo, hi};
  bf16x2_t b = __builtin_convertvector(v, bf16x2_t);
  return __builtin_bit_cast(unsigned, b);
}
DI f32x4 mfma16(bf16x8 a, bf16x8 b, f32x4 c) { return __builtin_amdgcn_mfma_f32_16x16x32_bf16(a, b, c, 0, 0, 0); }
DI rsrc_t mkrsrc(const void* p, unsigned bytes) { return __builtin_amdgcn_make_buffer_rsrc((void*)p, (short)0, (int)bytes, 0x00020000); }
DI void dma1(rsrc_t r, unsigned lds_addr, unsigned voff, unsigned soff) {
  asm volatile("s_mov_b32 m0, %0\n\ts_nop 0\n\tbuffer_load_dwordx4 %1, %2, %3 offen" NTK2 " lds" :: "s"(lds_addr), "v"(voff), "s"(r), "s"(soff) : "memory", "m0");
}
DI void dma1w(rsrc_t r, unsigned lds_addr, unsigned voff, unsigned soff) {
  asm volatile("s_mov_b32 m0, %0\n\ts_nop 0\n\tbuffer_load_dwordx4 %1, %2, %3 offen lds" :: "s"(lds_addr), "v"(voff), "s"(r), "s"(soff) : "memory", "m0");
}
DI void dma4(rsrc_t r, unsigned lds_addr, unsigned v0, unsigned v1, unsigned v2, unsigned v3, unsigned soff) {
  asm volatile("s_mov_b32 m0, %0\n\ts_nop 0\n\t"
               "buffer_load_dwordx4 %1, %5, %6 offen nt lds\n\t"
               "buffer_load_dwordx4 %2, %5, %6 offen offset:1024 nt lds\n\t"
               "buffer_load_dwordx4 %3, %5, %6 offen offset:2048 nt lds\n\t"
               "buffer_load_dwordx4 %4, %5, %6 offen offset:3072 nt lds"
               :: "s"(lds_addr), "v"(v0), "v"(v1), "v"(v2), "v"(v3), "s"(r), "s"(soff) : "memory", "m0");
}
DI void dma4s(rsrc_t r, unsigned lds_addr, unsigned v0, unsigned v1, unsigned s0, unsigned s1, unsigned s2, unsigned s3) {
  asm volatile("s_mov_b32 m0, %0\n\ts_nop 0\n\t"
               "buffer_load_dwordx4 %1, %3, %4 offen" NTW " lds\n\t"
               "buffer_load_dwordx4 %1, %3, %5 offen offset:1024" NTW " lds\n\t"
               "buffer_load_dwordx4 %2, %3, %6 offen offset:2048" NTW " lds\n\t"
               "buffer_load_dwordx4 %2, %3, %7 offen offset:3072" NTW " lds"
               :: "s"(lds_addr), "v"(v0), "v"(v1), "s"(r), "s"(s0), "s"(s1 - 1024u), "s"(s2 - 2048u), "s"(s3 - 3072u) : "memory", "m0");
}
DI void dma4d(rsrc_t r, unsigned lds_addr, unsigned v0, unsigned v1, unsigned s0, unsigned s1, unsigned s2, unsigned s3) {
  asm volatile("s_mov_b32 m0, %0\n\ts_nop 0\n\t"
               "buffer_load_dwordx4 %1, %3, %4 offen lds\n\t"
               "buffer_load_dwordx4 %2, %3, %5 offen offset:1024 lds\n\t"
               "buffer_load_dwordx4 %1, %3, %6 offen offset:2048 lds\n\t"
               "buffer_load_dwordx4 %2, %3, %7 offen offset:3072 lds"
               :: "s"(lds_addr), "v"(v0), "v"(v1), "s"(r), "s"(s0), "s"(s1 - 1024u), "s"(s2 - 2048u), "s"(s3 - 3072u) : "memory", "m0");
}
DI void dma3s(rsrc_t r, unsigned lds_addr, unsigned v0, unsigned s0, unsigned s1, unsigned s2) {
  asm volatile("s_mov_b32 m0, %0\n\ts_nop 0\n\t"
               "buffer_load_dwordx4 %1, %2, %3 offen" NTW " lds\n\t"
               "buffer_load_dwordx4 %1, %2, %4 offen offset:1024" NTW " lds\n\t"
               "buffer_load_dwordx4 %1, %2, %5 offen offset:2048" NTW " lds"
               :: "s"(lds_addr), "v"(v0), "s"(r), "s"(s0), "s"(s1 - 1024u), "s"(s2 - 2048u) : "memory", "m0");
}
DI void dma1x(rsrc_t r, unsigned lds_addr, unsigned voff, unsigned soff) {
  asm volatile("s_mov_b32 m0, %0\n\ts_nop 0\n\tbuffer_load_dwordx4 %1, %2, %3 offen" NTW " lds" :: "s"(lds_addr), "v"(voff), "s"(r), "s"(soff) : "memory", "m0");
}
DI void dma3(rsrc_t r, unsigned lds_addr, unsigned v0, unsigned v1, unsigned soff) {
  asm volatile("s_mov_b32 m0, %0\n\ts_nop 0\n\t"
               "buffer_load_dwordx4 %1, %3, %4 offen" NTK2 " lds\n\t"
               "buffer_load_dwordx4 %2, %3, %4 offen offset:1024" NTK2 " lds\n\t"
               "buffer_load_dwordx4 %1, %3, %4 offen offset:2048" NTK2 " lds"
               :: "s"(lds_addr), "v"(v0), "v"(v1), "s"(r), "s"(soff) : "memory", "m0");
}
DI bf16x8 bload_asm(rsrc_t r, unsigned voff, unsigned soff) {
  bf16x8 v;
  asm volatile("buffer_load_dwordx4 %0, %1, %2, %3 offen" NTW : "=v"(v) : "v"(voff), "s"(r), "s"(soff) : "memory");
  return v;
}
DI bf16x8 lds_rd16(unsigned addr) { return *lp<lds_bf16x8>(addr); }
DI bf16x8 lds_tr2(unsigned a0, unsigned a1) {
  s16x4 lo = __builtin_amdgcn_ds_read_tr16_b64_v4i16(lp<lds_s16x4>(a0));
  s16x4 hi = __builtin_amdgcn_ds_read_tr16_b64_v4i16(lp<lds_s16x4>(a1));
  return __builtin_shufflevector(lo, hi, 0, 1, 2, 3, 4, 5, 6, 7);
}
DI float xmax_groups(float x) {
  auto a = __builtin_amdgcn_permlane16_swap(__float_as_uint(x), __float_as_uint(x), false, false);
  x = fmaxf(__uint_as_float(a[0]), __uint_as_float(a[1]));
  auto b = __builtin_amdgcn_permlane32_swap(__float_as_uint(x), __float_as_uint(x), false, false);
  return fmaxf(__uint_as_float(b[0]), __uint_as_float(b[1]));
}
DI float xsum_groups(float x) {
  auto a = __builtin_amdgcn_permlane16_swap(__float_as_uint(x), __float_as_uint(x), false, false);
  x = __uint_as_float(a[0]) + __uint_as_float(a[1]);
  auto b = __builtin_amdgcn_permlane32_swap(__float_as_uint(x), __float_as_uint(x), false, false);
  return __uint_as_float(b[0]) + __uint_as_float(b[1]);
}
#define STAMP(i)
template <int N> DI void waitv() { asm volatile("s_waitcnt vmcnt(%0)" :: "i"(N) : "memory"); }
#define WAITL0() asm volatile("s_waitcnt lgkmcnt(0)" ::: "memory")
#define BARRIER() asm volatile("s_waitcnt lgkmcnt(0)\n\ts_barrier" ::: "memory")
#define SBAR() __builtin_amdgcn_sched_barrier(0)

constexpr int K1_LDS = 81920;
#define PFD 6
#define IC(x) std::integral_constant<int, x>{}

__global__ __launch_bounds__(256, 2)
void k_attn(const u16* __restrict__ xA, const u16* __restrict__ kA, const u16* __restrict__ vA, const u16* __restrict__ wqA,
            const u16* __restrict__ xB, const u16* __restrict__ kB, const u16* __restrict__ vB, const u16* __restrict__ wqB,
            const u16* __restrict__ woA, const u16* __restrict__ woB,
            u16* __restrict__ o_ws, unsigned long long* __restrict__ dbg) {
  extern __shared__ __attribute__((aligned(16))) char smem[];
  const unsigned lb = (unsigned)(uintptr_t)smem;
  const int bid = blockIdx.x;
  const int xcd = bid & 7, idx = bid >> 3;
  const int h = (xcd & 3) * 8 + (idx & 7), b = idx >> 3, br = xcd >> 2;
  const int tid = threadIdx.x, lane = tid & 63;
  const int wid = __builtin_amdgcn_readfirstlane(tid >> 6);
  const int l15 = lane & 15, g = lane >> 4, q4 = l15 >> 2, p4 = lane & 3;
  const unsigned L_XS = lb + 29696, L_W = lb + 57344, L_Q = lb + 65536;
#define LT(i)
  LT(0);
#define AS0()
#define AS(i)

  if (wid == 3) {
    const rsrc_t rW = mkrsrc((br ? wqB : wqA) + h * DH, (unsigned)(DM * HD * 2 - h * DH * 2));
    const rsrc_t rK = mkrsrc((br ? kB : kA) + (size_t)(b * NH + h) * SK * DH, SK * DH * 2);
    const rsrc_t rV = mkrsrc((br ? vB : vA) + (size_t)(b * NH + h) * SK * DH, SK * DH * 2);
    unsigned kvo[4], vvo[2];
#pragma unroll
    for (int p = 0; p < 4; ++p) kvo[p] = (unsigned)(g * 256 + ((l15 ^ (4 * p + g)) * 16));
#pragma unroll
    for (int p = 0; p < 2; ++p) vvo[p] = (unsigned)(g * 256 + ((l15 ^ ((4 * p + g) << 1)) * 16));
    auto load_item = [&](int n, int slot) {
      const unsigned la = lb + slot * 16384, so = (unsigned)(n >> 1) * 16384;
#pragma unroll
      for (int q = 0; q < 4; ++q) {
        if (n & 1) dma4(rV, la + q * 4096, vvo[0], vvo[1], vvo[0], vvo[1], so + q * 4096);
        else       dma4(rK, la + q * 4096, kvo[0], kvo[1], kvo[2], kvo[3], so + q * 4096);
      }
    };
    const unsigned wv0 = (unsigned)(g * (HD * 2) + ((l15 ^ (g << 1)) * 16));
    const unsigned wv1 = (unsigned)(g * (HD * 2) + ((l15 ^ ((g | 4) << 1)) * 16));
    auto wload = [&](int j) {
      const unsigned la = L_W + (unsigned)(j % 3) * 8192, so = (unsigned)j * 32 * (HD * 2);
      dma4s(rW, la, wv0, wv1, so, so + 4 * (HD * 2), so + 8 * (HD * 2), so + 12 * (HD * 2));
      dma4s(rW, la + 4096, wv0, wv1, so + 16 * (HD * 2), so + 20 * (HD * 2), so + 24 * (HD * 2), so + 28 * (HD * 2));
    };
    wload(0); wload(1); wload(2);
    load_item(0, 0);
    waitv<32>();
    BARRIER();
    auto lstep = [&](auto I) { constexpr int i = I;
      if constexpr (i == 0) AS0();
      if constexpr (i + 2 < 20) waitv<8>(); else waitv<0>();
      AS(0);
      BARRIER();
      AS(1);
      if constexpr (i + 3 < 20) wload(i + 3);
      AS(2);
    };
#define LS(i) lstep(IC(i))
    LS(0); LS(1); LS(2); LS(3); LS(4); LS(5); LS(6); LS(7); LS(8); LS(9);
    LS(10); LS(11); LS(12); LS(13); LS(14); LS(15); LS(16); LS(17); LS(18); LS(19);
#undef LS
    BARRIER();
    load_item(1, 1);
    load_item(2, 2);
    load_item(3, 3);
    BARRIER();
    load_item(4, 4);
    int slot = 0;
    for (int n = 0; n < 12; ++n) {
      if (n == 0) waitv<63>(); else waitv<48>();
      BARRIER();
      if (n >= 1) load_item(n + 4, slot == 0 ? 4 : slot - 1);
      slot = slot == 4 ? 0 : slot + 1;
    }
    {
      const rsrc_t rWo = mkrsrc((bid < 256 ? woA : woB) + (size_t)(bid & 255) * 10240, 20480);
      bf16x8 dm[20];
#pragma unroll
      for (int i = 0; i < 20; ++i) dm[i] = bload_asm(rWo, lane * 16, (unsigned)i * 1024);
      waitv<63>(); BARRIER();
      waitv<52>(); BARRIER();
      waitv<36>(); BARRIER();
      waitv<20>(); BARRIER();
      waitv<0>();
#pragma unroll
      for (int i = 0; i < 20; ++i) asm volatile("" :: "v"(dm[i]));
    }
    BARRIER();
    BARRIER();
  } else {
    {
      const rsrc_t rWa = mkrsrc((br ? wqB : wqA) + (xcd & 3) * 8 * DH, (unsigned)(DM * HD * 2 - (xcd & 3) * 8 * DH * 2));
      const rsrc_t rXa = mkrsrc((br ? xB : xA), NB * SQ * DM * 2);
      const unsigned dump = lb + 16384 + wid * 1024, lv = lane * 16;
#pragma unroll
      for (int i = 0; i < 7; ++i) { const unsigned p = wid * 7 + i; if (p < 20) dma1w(rWa, dump, lv, (unsigned)(idx * 10 + (p >> 1)) * (HD * 2) + (p & 1) * 1024); }
#pragma unroll
      for (int i = 0; i < 8; ++i) { const unsigned p = wid * 8 + i; if (p < 23) dma1w(rXa, dump, lv, (unsigned)idx * 23040 + p * 1024); }
    }
    f32x4 qa[8][3];
#pragma unroll
    for (int j = 0; j < 8; ++j) { qa[j][0] = f32x4{0, 0, 0, 0}; qa[j][1] = f32x4{0, 0, 0, 0}; qa[j][2] = f32x4{0, 0, 0, 0}; }
    {
      const unsigned xr = L_XS + (48 * wid + l15) * 64 + ((g ^ ((0x1320u >> (4 * (l15 >> 2))) & 3)) * 16);
      const unsigned wb = L_W + (unsigned)((8 * g + q4) * 256 + 8 * (p4 & 1) + 16 * (p4 >> 1)) + 32u * (unsigned)(q4 | ((g & 1) << 2));
      bf16x8 fx[2][3], fa[2][8];
      auto fread = [&](auto J) { constexpr int j = J, p = j & 1;
        fx[p][0] = lds_rd16(xr + (j % 3) * 9216); fx[p][1] = lds_rd16(xr + (j % 3) * 9216 + 1024); fx[p][2] = lds_rd16(xr + (j % 3) * 9216 + 2048);
#pragma unroll
        for (int d = 0; d < 8; ++d) { const unsigned a0 = (wb ^ (32u * d)) + (j % 3) * 8192; fa[p][d] = lds_tr2(a0, a0 + 1024); } };
      const rsrc_t rX = mkrsrc((br ? xB : xA) + (size_t)(b * SQ + 48 * wid) * DM, 48 * DM * 2);
      const unsigned xv = (unsigned)((lane >> 2) * (DM * 2) + (((lane & 3) ^ ((0x1320u >> (4 * ((lane >> 4) & 3))) & 3)) * 16));
      auto xload = [&](int j) {
        const unsigned so = (unsigned)j * 64;
        dma3s(rX, L_XS + (unsigned)(j % 3) * 9216 + wid * 3072, xv, so, so + 20480, so + 2 * 20480); };
      xload(0); xload(1); xload(2);
      waitv<6>();
      BARRIER();
      LT(1);
      fread(IC(0));
      auto cstep = [&](auto I) { constexpr int i = I, p = i & 1;
        if constexpr (i == 0) AS0();
        if constexpr (i + 2 < 20) waitv<3>(); else waitv<0>();
        AS(0);
        BARRIER();
        AS(1);
        if constexpr (i + 3 < 20) xload(i + 3);
        AS(3);
        if constexpr (i + 1 < 20) fread(IC(i + 1));
        AS(2);
#pragma unroll
        for (int j = 0; j < 8; ++j) {
          qa[j][0] = mfma16(fa[p][j], fx[p][0], qa[j][0]);
          qa[j][1] = mfma16(fa[p][j], fx[p][1], qa[j][1]);
          qa[j][2] = mfma16(fa[p][j], fx[p][2], qa[j][2]);
        }
        AS(4); };
#define CS(i) cstep(IC(i))
      CS(0); CS(1); CS(2); CS(3); CS(4); CS(5); CS(6); CS(7); CS(8); CS(9);
      CS(10); CS(11); CS(12); CS(13); CS(14); CS(15); CS(16); CS(17); CS(18); CS(19);
#undef CS
    }
    LT(2);
    waitv<0>();
    BARRIER();
    bf16x8 qf[3][4];
    {
      const unsigned wreg = L_Q + wid * 4096;
      const unsigned wq_l = wreg + (unsigned)((l15 >> 3) * 2048 + (l15 & 7) * 16 + (g >> 1) * 128 + (g & 1) * 8);
      const unsigned rq_l = wreg + (unsigned)((l15 >> 3) * 2048 + (l15 & 7) * 16 + g * 128);
#pragma unroll
      for (int qb = 0; qb < 3; ++qb) {
#pragma unroll
        for (int j = 0; j < 8; ++j) {
          u32x2 w = {cvtpk(qa[j][qb][0] * QSCALE, qa[j][qb][1] * QSCALE), cvtpk(qa[j][qb][2] * QSCALE, qa[j][qb][3] * QSCALE)};
          *lp<lds_u32x2>(wq_l + j * 256) = w;
        }
        WAITL0();
#pragma unroll
        for (int s = 0; s < 4; ++s) qf[qb][s] = lds_rd16(rq_l + s * 512);
        WAITL0();
      }
    }
    BARRIER();
    LT(3);
    f32x4 O[3][8];
#pragma unroll
    for (int d = 0; d < 8; ++d) { O[0][d] = f32x4{0, 0, 0, 0}; O[1][d] = f32x4{0, 0, 0, 0}; O[2][d] = f32x4{0, 0, 0, 0}; }
    f32x4 L[3] = {f32x4{0, 0, 0, 0}, f32x4{0, 0, 0, 0}, f32x4{0, 0, 0, 0}};
    const bf16x8 ones = {0x3F80, 0x3F80, 0x3F80, 0x3F80, 0x3F80, 0x3F80, 0x3F80, 0x3F80};
    f32x4 negm[3] = {f32x4{0, 0, 0, 0}, f32x4{0, 0, 0, 0}, f32x4{0, 0, 0, 0}};
    unsigned koff[4];
#pragma unroll
    for (int s = 0; s < 4; ++s) koff[s] = lb + 256u * l15 + 16u * ((unsigned)(4 * s + g) ^ (unsigned)l15);
    const unsigned vbase = lb + 256u * (4 * g + q4) + 8u * (p4 & 1) + 16u * (p4 >> 1) + 32u * (unsigned)(4 * (g & 1) + q4);
    f32x4 S[3][4];
    bf16x8 P[3][2];
    int slot = 0;
#define BS(i)
    for (int t = 0; t < 8; ++t) {
      BARRIER();
      BS(0);
      {
        const unsigned ka = slot * 16384;
        bf16x8 F[PFD];
#pragma unroll
        for (int f = 0; f < PFD; ++f) F[f] = lds_rd16(koff[f & 3] + ka + (f >> 2) * 4096);
#pragma unroll
        for (int f = 0; f < 16; ++f) {
          const int kb = f >> 2, s = f & 3;
          const bf16x8 A = F[f % PFD];
          if (f + PFD < 16) F[f % PFD] = lds_rd16(koff[(f + PFD) & 3] + ka + ((f + PFD) >> 2) * 4096);
#pragma unroll
          for (int qb = 0; qb < 3; ++qb) S[qb][kb] = mfma16(A, qf[qb][s], s == 0 ? negm[qb] : S[qb][kb]);
          __builtin_amdgcn_sched_barrier(0);
        }
        BS(1);
        float mx[3];
#pragma unroll
        for (int qb = 0; qb < 3; ++qb) {
          float m = S[qb][0][0];
#pragma unroll
          for (int kb = 0; kb < 4; ++kb)
#pragma unroll
            for (int r = 0; r < 4; ++r) m = fmaxf(m, S[qb][kb][r]);
          mx[qb] = xmax_groups(m);
        }
        const bool need = (t == 0) || (mx[0] > THR) || (mx[1] > THR) || (mx[2] > THR);
        if (__any(need)) {
#pragma unroll
          for (int qb = 0; qb < 3; ++qb) {
            const float dl = (t == 0) ? mx[qb] : fmaxf(mx[qb], 0.f);
            const float al = (t == 0) ? 1.f : __builtin_amdgcn_exp2f(-dl);
            negm[qb] -= dl; L[qb] *= al;
#pragma unroll
            for (int kb = 0; kb < 4; ++kb) S[qb][kb] -= dl;
#pragma unroll
            for (int d = 0; d < 8; ++d) O[qb][d] *= al;
          }
        }
#pragma unroll
        for (int qb = 0; qb < 3; ++qb) {
#pragma unroll
          for (int kb = 0; kb < 4; ++kb)
#pragma unroll
            for (int r = 0; r < 4; ++r) S[qb][kb][r] = __builtin_amdgcn_exp2f(S[qb][kb][r]);
#pragma unroll
          for (int jj = 0; jj < 2; ++jj) {
            u32x4 w = {cvtpk(S[qb][2 * jj][0], S[qb][2 * jj][1]), cvtpk(S[qb][2 * jj][2], S[qb][2 * jj][3]),
                       cvtpk(S[qb][2 * jj + 1][0], S[qb][2 * jj + 1][1]), cvtpk(S[qb][2 * jj + 1][2], S[qb][2 * jj + 1][3])};
            P[qb][jj] = __builtin_bit_cast(bf16x8, w);
            L[qb] = mfma16(ones, P[qb][jj], L[qb]);
          }
        }
      }
      slot = slot == 4 ? 0 : slot + 1;
      { bf16x8 &p0 = P[0][0], &p1 = P[0][1], &p2 = P[1][0], &p3 = P[1][1], &p4_ = P[2][0], &p5 = P[2][1];
        asm volatile("" : "+v"(p0), "+v"(p1), "+v"(p2), "+v"(p3), "+v"(p4_), "+v"(p5)); }
      BS(2);
      BARRIER();
      BS(3);
      {
        const unsigned va = vbase + slot * 16384;
        bf16x8 F[PFD];
#pragma unroll
        for (int f = 0; f < PFD; ++f) { const unsigned a0 = (va ^ (32u * (f & 7))) + (f >> 3) * 8192; F[f] = lds_tr2(a0, a0 + 4096); }
#pragma unroll
        for (int f = 0; f < 16; ++f) {
          const int jj = f >> 3, d = f & 7;
          const bf16x8 A = F[f % PFD];
          if (f + PFD < 16) { const unsigned a0 = (va ^ (32u * ((f + PFD) & 7))) + ((f + PFD) >> 3) * 8192; F[f % PFD] = lds_tr2(a0, a0 + 4096); }
#pragma unroll
          for (int qb = 0; qb < 3; ++qb) O[qb][d] = mfma16(A, P[qb][jj], O[qb][d]);
          __builtin_amdgcn_sched_barrier(0);
        }
      }
      slot = slot == 4 ? 0 : slot + 1;
      BS(4);
    }
    BARRIER();
    LT(4);
    {
      int tz = threadIdx.x; asm volatile("" : "+v"(tz));
      const int lane = tz & 63, l15 = lane & 15, g = lane >> 4;
#pragma unroll
      for (int qb = 0; qb < 3; ++qb) {
        const float il = 1.0f / L[qb][0];
        const unsigned row = (unsigned)((3 * wid + qb) * 16 + l15);
#pragma unroll
        for (int d = 0; d < 8; ++d) {
          u32x2 w = {cvtpk(O[qb][d][0] * il, O[qb][d][1] * il), cvtpk(O[qb][d][2] * il, O[qb][d][3] * il)};
          *lp<lds_u32x2>(lb + 256u * row + 16u * ((unsigned)(2 * d + (g >> 1)) ^ l15) + 8u * (g & 1)) = w;
        }
      }
    }
    BARRIER();
  }
  {
    int tz = threadIdx.x; asm volatile("" : "+v"(tz));
    u16* og = o_ws + ((size_t)(br * 64 + 2 * h) * MROWS + (size_t)b * SQ) * 64;
#pragma unroll
    for (int i = 0; i < 9; ++i) {
      const unsigned row = (unsigned)(i * 16 + (tz >> 4)), c = tz & 15;
      u32x4 v = *lp<lds_u32x4>(lb + 256u * row + 16u * (c ^ (row & 15)));
      *reinterpret_cast<u32x4*>(og + (size_t)(c >> 3) * MROWS * 64 + (size_t)row * 64 + (c & 7) * 8) = v;
    }
  }
}

constexpr int K2_STAGE = 40960, K2_LDS = K2_STAGE * 4;

__global__ __launch_bounds__(512, 1)
void k_oproj(const u16* __restrict__ o_ws, const u16* __restrict__ woA, const u16* __restrict__ woB, u16* __restrict__ out, unsigned long long* __restrict__ dbg) {
  extern __shared__ __attribute__((aligned(16))) char smem[];
  const unsigned lb = (unsigned)(uintptr_t)smem;
  const int tid = threadIdx.x, lane = tid & 63;
  const int wv = __builtin_amdgcn_readfirstlane(tid >> 6), wid = wv & 3;
  const int bid = blockIdx.x;
  const int xcd = bid & 7, idx = bid >> 3;
  const int br = xcd >> 2, mt = (xcd & 3) * 3 + idx / 10, nt = idx % 10;
  const int l15 = lane & 15, g = lane >> 4, q4 = l15 >> 2, p4 = lane & 3;
  if (wv >= 4) {
    const int rot = ((xcd & 3) * 8) & 31;
    const rsrc_t rO = mkrsrc(o_ws + ((size_t)br * 64 * MROWS + (size_t)mt * 96) * 64, (unsigned)((64 * MROWS - mt * 96) * 128));
    const rsrc_t rW = mkrsrc((br ? woB : woA) + nt * 64, (unsigned)(HD * DM * 2 - nt * 128));
    unsigned ovo[2], wvo[2];
#pragma unroll
    for (int p = 0; p < 2; ++p) {
      ovo[p] = (unsigned)((lane >> 3) * 128 + (((lane & 7) ^ (4 * p + (lane >> 4))) * 16));
      wvo[p] = (unsigned)((lane >> 3) * (DM * 2) + (((lane & 7) ^ ((((lane >> 4) & 1) | (p << 1)) << 1)) * 16));
    }
    const unsigned ova = (wid & 1) ? ovo[1] : ovo[0], ovb = (wid & 1) ? ovo[0] : ovo[1];
    auto load_stage = [&](int c0) {
      const unsigned sb = lb + (c0 & 3) * K2_STAGE;
      const int c = (c0 + rot) & 31;
      dma3(rO, sb + wid * 3072, ova, ovb, (unsigned)(2 * c) * (MROWS * 128) + wid * 3072);
      dma3(rO, sb + 12288 + wid * 3072, ova, ovb, (unsigned)(2 * c + 1) * (MROWS * 128) + wid * 3072);
      const unsigned so = (unsigned)(c * 128 + 32 * wid) * (DM * 2);
      dma4d(rW, sb + 24576 + wid * 4096, wvo[0], wvo[1], so, so + 8 * (DM * 2), so + 16 * (DM * 2), so + 24 * (DM * 2));
    };
    load_stage(0); load_stage(1); load_stage(2);
    for (int c = 0; c < 32; ++c) {
      if (c < 30) waitv<20>(); else if (c < 31) waitv<10>(); else waitv<0>();
      BARRIER();
      if (c + 3 < 32) load_stage(c + 3);
    }
    BARRIER();
    BARRIER();
  } else {
    const int nh = wid & 1, kh = wid >> 1;
    f32x4 acc[2][6];
#pragma unroll
    for (int n = 0; n < 2; ++n)
#pragma unroll
      for (int m = 0; m < 6; ++m) acc[n][m] = f32x4{0, 0, 0, 0};
    const unsigned boff = (unsigned)(128 * l15 + 16 * ((4 * kh + g) ^ ((l15 >> 1) & 7)));
    unsigned aoff[2];
#pragma unroll
    for (int n = 0; n < 2; ++n) {
      const unsigned fl = (unsigned)(((q4 >> 1) & 1) | ((g & 1) << 1));
      aoff[n] = 24576u + 128u * (32 * kh + 8 * g + q4) + 32u * ((unsigned)(2 * nh + n) ^ fl) + 16u * (p4 >> 1) + 8u * (p4 & 1);
    }
    for (int c = 0; c < 32; ++c) {
      BARRIER();
      const unsigned sb = lb + (c & 3) * K2_STAGE;
      bf16x8 Bf[2][6], A[2][2];
#pragma unroll
      for (int sc = 0; sc < 2; ++sc) {
#pragma unroll
        for (int n = 0; n < 2; ++n) { const unsigned a0 = sb + aoff[n] + sc * 8192; A[sc][n] = lds_tr2(a0, a0 + 512); }
#pragma unroll
        for (int m = 0; m < 6; ++m) Bf[sc][m] = lds_rd16(sb + sc * 12288 + boff + m * 2048);
      }
#pragma unroll
      for (int sc = 0; sc < 2; ++sc)
#pragma unroll
        for (int n = 0; n < 2; ++n)
#pragma unroll
          for (int m = 0; m < 6; ++m) acc[n][m] = mfma16(A[sc][n], Bf[sc][m], acc[n][m]);
    }
    BARRIER();
    if (kh == 1) {
#pragma unroll
      for (int n = 0; n < 2; ++n)
#pragma unroll
        for (int m = 0; m < 6; ++m) *lp<lds_f32x4>(lb + (unsigned)(((nh * 12) + n * 6 + m) * 1024) + lane * 16) = acc[n][m];
    }
    BARRIER();
    if (kh == 0) {
      u16* og = out + (size_t)(mt * 96) * (2 * DM) + br * DM + nt * 64;
#pragma unroll
      for (int n = 0; n < 2; ++n)
#pragma unroll
        for (int m = 0; m < 6; ++m) {
          f32x4 v = acc[n][m] + *lp<lds_f32x4>(lb + (unsigned)(((nh * 12) + n * 6 + m) * 1024) + lane * 16);
          u32x2 w = {cvtpk(v[0], v[1]), cvtpk(v[2], v[3])};
          *reinterpret_cast<u32x2*>(og + (size_t)(m * 16 + l15) * (2 * DM) + (2 * nh + n) * 16 + 4 * g) = w;
        }
    }
  }
}

extern "C" void kernel_launch(void* const* d_in, const int* in_sizes, int n_in,
                              void* d_out, int out_size, void* d_ws, size_t ws_size,
                              hipStream_t stream) {
  const u16* xA = (const u16*)d_in[0];
  const u16* kA = (const u16*)d_in[1];
  const u16* vA = (const u16*)d_in[2];
  const u16* wqA = (const u16*)d_in[3];
  const u16* woA = (const u16*)d_in[4];
  const u16* xB = (const u16*)d_in[5];
  const u16* kB = (const u16*)d_in[6];
  const u16* vB = (const u16*)d_in[7];
  const u16* wqB = (const u16*)d_in[8];
  const u16* woB = (const u16*)d_in[9];
  u16* o_ws = (u16*)d_ws;
  unsigned long long* dbg = (unsigned long long*)((char*)d_ws + (200u << 20));
  (void)hipFuncSetAttribute((const void*)k_attn, hipFuncAttributeMaxDynamicSharedMemorySize, K1_LDS);
  (void)hipFuncSetAttribute((const void*)k_oproj, hipFuncAttributeMaxDynamicSharedMemorySize, K2_LDS);
  k_attn<<<K1GRID, 256, K1_LDS, stream>>>(xA, kA, vA, wqA, xB, kB, vB, wqB, woA, woB, o_ws, dbg);
  k_oproj<<<240, 512, K2_LDS, stream>>>(o_ws, woA, woB, (u16*)d_out, dbg + 512 * 64);
}
```

```cpp
#include <hip/hip_runtime.h>
#include <hip/hip_bf16.h>
#include <stdint.h>
#include <type_traits>

typedef __attribute__((ext_vector_type(8))) short bf16x8;
typedef __attribute__((ext_vector_type(4))) short s16x4;
typedef __attribute__((ext_vector_type(4))) float f32x4;
typedef __attribute__((ext_vector_type(2))) float f32x2;
typedef __attribute__((ext_vector_type(2))) __bf16 bf16x2_t;
typedef __attribute__((ext_vector_type(4))) unsigned u32x4;
typedef __attribute__((ext_vector_type(2))) unsigned u32x2;
typedef unsigned short u16;
typedef __attribute__((address_space(3))) bf16x8 lds_bf16x8;
typedef __attribute__((address_space(3))) s16x4 lds_s16x4;
typedef __attribute__((address_space(3))) f32x4 lds_f32x4;
typedef __attribute__((address_space(3))) u32x4 lds_u32x4;
typedef __attribute__((address_space(3))) u32x2 lds_u32x2;
typedef __attribute__((address_space(3))) float lds_f32;
typedef __amdgpu_buffer_rsrc_t rsrc_t;

#define DI __device__ __forceinline__
template <class T> DI T* lp(unsigned a) { return (T*)(uintptr_t)a; }

constexpr int NB = 8, SQ = 144, DM = 640, NH = 32, SK = 512, DH = 128;
constexpr int HD = NH * DH;
constexpr int MROWS = NB * SQ;
constexpr float QSCALE = 0.088388347648318440f * 1.4426950408889634f;
constexpr float THR = 8.0f;

DI unsigned cvtpk(float lo, float hi) {
  f32x2 v = {lo, hi};
  bf16x2_t b = __builtin_convertvector(v, bf16x2_t);
  return __builtin_bit_cast(unsigned, b);
}
DI f32x4 mfma16(bf16x8 a, bf16x8 b, f32x4 c) { return __builtin_amdgcn_mfma_f32_16x16x32_bf16(a, b, c, 0, 0, 0); }
DI rsrc_t mkrsrc(const void* p, unsigned bytes) { return __builtin_amdgcn_make_buffer_rsrc((void*)p, (short)0, (int)bytes, 0x00020000); }
DI void dma1w(rsrc_t r, unsigned lds_addr, unsigned voff, unsigned soff) {
  asm volatile("s_mov_b32 m0, %0\n\ts_nop 0\n\tbuffer_load_dwordx4 %1, %2, %3 offen lds" :: "s"(lds_addr), "v"(voff), "s"(r), "s"(soff) : "memory", "m0");
}
DI void dma4(rsrc_t r, unsigned lds_addr, unsigned v0, unsigned v1, unsigned v2, unsigned v3, unsigned soff) {
  asm volatile("s_mov_b32 m0, %0\n\ts_nop 0\n\t"
               "buffer_load_dwordx4 %1, %5, %6 offen nt lds\n\t"
               "buffer_load_dwordx4 %2, %5, %6 offen offset:1024 nt lds\n\t"
               "buffer_load_dwordx4 %3, %5, %6 offen offset:2048 nt lds\n\t"
               "buffer_load_dwordx4 %4, %5, %6 offen offset:3072 nt lds"
               :: "s"(lds_addr), "v"(v0), "v"(v1), "v"(v2), "v"(v3), "s"(r), "s"(soff) : "memory", "m0");
}
DI void dma4s(rsrc_t r, unsigned lds_addr, unsigned v0, unsigned v1, unsigned s0, unsigned s1, unsigned s2, unsigned s3) {
  asm volatile("s_mov_b32 m0, %0\n\ts_nop 0\n\t"
               "buffer_load_dwordx4 %1, %3, %4 offen lds\n\t"
               "buffer_load_dwordx4 %1, %3, %5 offen offset:1024 lds\n\t"
               "buffer_load_dwordx4 %2, %3, %6 offen offset:2048 lds\n\t"
               "buffer_load_dwordx4 %2, %3, %7 offen offset:3072 lds"
               :: "s"(lds_addr), "v"(v0), "v"(v1), "s"(r), "s"(s0), "s"(s1 - 1024u), "s"(s2 - 2048u), "s"(s3 - 3072u) : "memory", "m0");
}
DI void dma4d(rsrc_t r, unsigned lds_addr, unsigned v0, unsigned v1, unsigned s0, unsigned s1, unsigned s2, unsigned s3) {
  asm volatile("s_mov_b32 m0, %0\n\ts_nop 0\n\t"
               "buffer_load_dwordx4 %1, %3, %4 offen lds\n\t"
               "buffer_load_dwordx4 %2, %3, %5 offen offset:1024 lds\n\t"
               "buffer_load_dwordx4 %1, %3, %6 offen offset:2048 lds\n\t"
               "buffer_load_dwordx4 %2, %3, %7 offen offset:3072 lds"
               :: "s"(lds_addr), "v"(v0), "v"(v1), "s"(r), "s"(s0), "s"(s1 - 1024u), "s"(s2 - 2048u), "s"(s3 - 3072u) : "memory", "m0");
}
DI void dma3s(rsrc_t r, unsigned lds_addr, unsigned v0, unsigned s0, unsigned s1, unsigned s2) {
  asm volatile("s_mov_b32 m0, %0\n\ts_nop 0\n\t"
               "buffer_load_dwordx4 %1, %2, %3 offen lds\n\t"
               "buffer_load_dwordx4 %1, %2, %4 offen offset:1024 lds\n\t"
               "buffer_load_dwordx4 %1, %2, %5 offen offset:2048 lds"
               :: "s"(lds_addr), "v"(v0), "s"(r), "s"(s0), "s"(s1 - 1024u), "s"(s2 - 2048u) : "memory", "m0");
}
DI void dma3(rsrc_t r, unsigned lds_addr, unsigned v0, unsigned v1, unsigned soff) {
  asm volatile("s_mov_b32 m0, %0\n\ts_nop 0\n\t"
               "buffer_load_dwordx4 %1, %3, %4 offen lds\n\t"
               "buffer_load_dwordx4 %2, %3, %4 offen offset:1024 lds\n\t"
               "buffer_load_dwordx4 %1, %3, %4 offen offset:2048 lds"
               :: "s"(lds_addr), "v"(v0), "v"(v1), "s"(r), "s"(soff) : "memory", "m0");
}
DI bf16x8 bload_asm(rsrc_t r, unsigned voff, unsigned soff) {
  bf16x8 v;
  asm volatile("buffer_load_dwordx4 %0, %1, %2, %3 offen" : "=v"(v) : "v"(voff), "s"(r), "s"(soff) : "memory");
  return v;
}
DI bf16x8 lds_rd16(unsigned addr) { return *lp<lds_bf16x8>(addr); }
DI bf16x8 lds_tr2(unsigned a0, unsigned a1) {
  s16x4 lo = __builtin_amdgcn_ds_read_tr16_b64_v4i16(lp<lds_s16x4>(a0));
  s16x4 hi = __builtin_amdgcn_ds_read_tr16_b64_v4i16(lp<lds_s16x4>(a1));
  return __builtin_shufflevector(lo, hi, 0, 1, 2, 3, 4, 5, 6, 7);
}
DI float xmax_groups(float x) {
  auto a = __builtin_amdgcn_permlane16_swap(__float_as_uint(x), __float_as_uint(x), false, false);
  x = fmaxf(__uint_as_float(a[0]), __uint_as_float(a[1]));
  auto b = __builtin_amdgcn_permlane32_swap(__float_as_uint(x), __float_as_uint(x), false, false);
  return fmaxf(__uint_as_float(b[0]), __uint_as_float(b[1]));
}
template <int N> DI void waitv() { asm volatile("s_waitcnt vmcnt(%0)" :: "i"(N) : "memory"); }
#define WAITL0() asm volatile("s_waitcnt lgkmcnt(0)" ::: "memory")
#define BARRIER() asm volatile("s_waitcnt lgkmcnt(0)\n\ts_barrier" ::: "memory")

constexpr int K1_LDS = 81920;
constexpr int PFD = 6;
#define IC(x) std::integral_constant<int, x>{}

__global__ __launch_bounds__(256, 2)
void k_attn(const u16* __restrict__ xA, const u16* __restrict__ kA, const u16* __restrict__ vA, const u16* __restrict__ wqA,
            const u16* __restrict__ xB, const u16* __restrict__ kB, const u16* __restrict__ vB, const u16* __restrict__ wqB,
            const u16* __restrict__ woA, const u16* __restrict__ woB,
            u16* __restrict__ o_ws) {
  extern __shared__ __attribute__((aligned(16))) char smem[];
  const unsigned lb = (unsigned)(uintptr_t)smem;
  const int bid = blockIdx.x;
  const int xcd = bid & 7, idx = bid >> 3;
  const int h = (xcd & 3) * 8 + (idx & 7), b = idx >> 3, br = xcd >> 2;
  const int tid = threadIdx.x, lane = tid & 63;
  const int wid = __builtin_amdgcn_readfirstlane(tid >> 6);
  const int l15 = lane & 15, g = lane >> 4, q4 = l15 >> 2, p4 = lane & 3;
  const unsigned L_XS = lb + 29696, L_W = lb + 57344, L_Q = lb + 65536;

  if (wid == 3) {
    const rsrc_t rW = mkrsrc((br ? wqB : wqA) + h * DH, (unsigned)(DM * HD * 2 - h * DH * 2));
    const rsrc_t rK = mkrsrc((br ? kB : kA) + (size_t)(b * NH + h) * SK * DH, SK * DH * 2);
    const rsrc_t rV = mkrsrc((br ? vB : vA) + (size_t)(b * NH + h) * SK * DH, SK * DH * 2);
    unsigned kvo[4], vvo[2];
#pragma unroll
    for (int p = 0; p < 4; ++p) kvo[p] = (unsigned)(g * 256 + ((l15 ^ (4 * p + g)) * 16));
#pragma unroll
    for (int p = 0; p < 2; ++p) vvo[p] = (unsigned)(g * 256 + ((l15 ^ ((4 * p + g) << 1)) * 16));
    auto load_item = [&](int n, int slot, int q0 = 0, int q1 = 4) {
      const unsigned la = lb + slot * 16384, so = (unsigned)(n >> 1) * 16384;
#pragma unroll
      for (int q = q0; q < q1; ++q) {
        if (n & 1) dma4(rV, la + q * 4096, vvo[0], vvo[1], vvo[0], vvo[1], so + q * 4096);
        else       dma4(rK, la + q * 4096, kvo[0], kvo[1], kvo[2], kvo[3], so + q * 4096);
      }
    };
    const unsigned wv0 = (unsigned)(g * (HD * 2) + ((l15 ^ (g << 1)) * 16));
    const unsigned wv1 = (unsigned)(g * (HD * 2) + ((l15 ^ ((g | 4) << 1)) * 16));
    auto wload = [&](int j) {
      const unsigned la = L_W + (unsigned)(j % 3) * 8192, so = (unsigned)j * 32 * (HD * 2);
      dma4s(rW, la, wv0, wv1, so, so + 4 * (HD * 2), so + 8 * (HD * 2), so + 12 * (HD * 2));
      dma4s(rW, la + 4096, wv0, wv1, so + 16 * (HD * 2), so + 20 * (HD * 2), so + 24 * (HD * 2), so + 28 * (HD * 2));
    };
    wload(0); wload(1); wload(2);
    load_item(0, 0);
    load_item(1, 1, 0, 3);
    waitv<44>();
    BARRIER();
    auto lstep = [&](auto I) { constexpr int i = I;
      if constexpr (i + 2 < 20) waitv<8>(); else waitv<0>();
      BARRIER();
      if constexpr (i + 3 < 20) wload(i + 3);
    };
#define LS(i) lstep(IC(i))
    LS(0); LS(1); LS(2); LS(3); LS(4); LS(5); LS(6); LS(7); LS(8); LS(9);
    LS(10); LS(11); LS(12); LS(13); LS(14); LS(15); LS(16); LS(17); LS(18); LS(19);
#undef LS
    BARRIER();
    load_item(1, 1, 3, 4);
    load_item(2, 2);
    load_item(3, 3);
    BARRIER();
    load_item(4, 4);
    int slot = 0;
    for (int n = 0; n < 12; ++n) {
      if (n == 0) waitv<63>(); else waitv<48>();
      BARRIER();
      if (n >= 1) load_item(n + 4, slot == 0 ? 4 : slot - 1);
      slot = slot == 4 ? 0 : slot + 1;
    }
    {
      const rsrc_t rWo = mkrsrc((bid < 256 ? woA : woB) + (size_t)(bid & 255) * 10240, 20480);
      bf16x8 dm[20];
#pragma unroll
      for (int i = 0; i < 20; ++i) dm[i] = bload_asm(rWo, lane * 16, (unsigned)i * 1024);
      waitv<63>(); BARRIER();
      waitv<52>(); BARRIER();
      waitv<36>(); BARRIER();
      waitv<20>(); BARRIER();
      waitv<0>();
#pragma unroll
      for (int i = 0; i < 20; ++i) asm volatile("" :: "v"(dm[i]));
    }
    BARRIER();
    BARRIER();
  } else {
    {
      const rsrc_t rWa = mkrsrc((br ? wqB : wqA) + (xcd & 3) * 8 * DH, (unsigned)(DM * HD * 2 - (xcd & 3) * 8 * DH * 2));
      const rsrc_t rXa = mkrsrc((br ? xB : xA), NB * SQ * DM * 2);
      const unsigned dump = lb + 28672, lv = lane * 16;
#pragma unroll
      for (int i = 0; i < 7; ++i) { const unsigned p = wid * 7 + i; if (p < 20) dma1w(rWa, dump, lv, (unsigned)(idx * 10 + (p >> 1)) * (HD * 2) + (p & 1) * 1024); }
#pragma unroll
      for (int i = 0; i < 8; ++i) { const unsigned p = wid * 8 + i; if (p < 23) dma1w(rXa, dump, lv, (unsigned)idx * 23040 + p * 1024); }
    }
    f32x4 qa[8][3];
#pragma unroll
    for (int j = 0; j < 8; ++j) { qa[j][0] = f32x4{0, 0, 0, 0}; qa[j][1] = f32x4{0, 0, 0, 0}; qa[j][2] = f32x4{0, 0, 0, 0}; }
    {
      const unsigned xr = L_XS + (48 * wid + l15) * 64 + ((g ^ ((0x1320u >> (4 * (l15 >> 2))) & 3)) * 16);
      const unsigned wb = L_W + (unsigned)((8 * g + q4) * 256 + 8 * (p4 & 1) + 16 * (p4 >> 1)) + 32u * (unsigned)(q4 | ((g & 1) << 2));
      bf16x8 fx[2][3], fa[2][8];
      auto fread = [&](auto J) { constexpr int j = J, p = j & 1;
        fx[p][0] = lds_rd16(xr + (j % 3) * 9216); fx[p][1] = lds_rd16(xr + (j % 3) * 9216 + 1024); fx[p][2] = lds_rd16(xr + (j % 3) * 9216 + 2048);
#pragma unroll
        for (int d = 0; d < 8; ++d) { const unsigned a0 = (wb ^ (32u * d)) + (j % 3) * 8192; fa[p][d] = lds_tr2(a0, a0 + 1024); } };
      const rsrc_t rX = mkrsrc((br ? xB : xA) + (size_t)(b * SQ + 48 * wid) * DM, 48 * DM * 2);
      const unsigned xv = (unsigned)((lane >> 2) * (DM * 2) + (((lane & 3) ^ ((0x1320u >> (4 * ((lane >> 4) & 3))) & 3)) * 16));
      auto xload = [&](int j) {
        const unsigned so = (unsigned)j * 64;
        dma3s(rX, L_XS + (unsigned)(j % 3) * 9216 + wid * 3072, xv, so, so + 20480, so + 2 * 20480); };
      xload(0); xload(1); xload(2);
      waitv<6>();
      BARRIER();
      fread(IC(0));
      auto cstep = [&](auto I) { constexpr int i = I, p = i & 1;
        if constexpr (i + 2 < 20) waitv<3>(); else waitv<0>();
        BARRIER();
        if constexpr (i + 1 < 20) fread(IC(i + 1));
        __builtin_amdgcn_sched_barrier(0);
#pragma unroll
        for (int j = 0; j < 8; ++j) {
          qa[j][0] = mfma16(fa[p][j], fx[p][0], qa[j][0]);
          qa[j][1] = mfma16(fa[p][j], fx[p][1], qa[j][1]);
          qa[j][2] = mfma16(fa[p][j], fx[p][2], qa[j][2]);
        }
        __builtin_amdgcn_sched_barrier(0);
        if constexpr (i + 3 < 20) xload(i + 3);
      };
#define CS(i) cstep(IC(i))
      CS(0); CS(1); CS(2); CS(3); CS(4); CS(5); CS(6); CS(7); CS(8); CS(9);
      CS(10); CS(11); CS(12); CS(13); CS(14); CS(15); CS(16); CS(17); CS(18); CS(19);
#undef CS
    }
    waitv<0>();
    BARRIER();
    bf16x8 qf[3][4];
    {
      const unsigned wreg = L_Q + wid * 4096;
      const unsigned wq_l = wreg + (unsigned)((l15 >> 3) * 2048 + (l15 & 7) * 16 + (g >> 1) * 128 + (g & 1) * 8);
      const unsigned rq_l = wreg + (unsigned)((l15 >> 3) * 2048 + (l15 & 7) * 16 + g * 128);
#pragma unroll
      for (int qb = 0; qb < 3; ++qb) {
#pragma unroll
        for (int j = 0; j < 8; ++j) {
          u32x2 w = {cvtpk(qa[j][qb][0] * QSCALE, qa[j][qb][1] * QSCALE), cvtpk(qa[j][qb][2] * QSCALE, qa[j][qb][3] * QSCALE)};
          *lp<lds_u32x2>(wq_l + j * 256) = w;
        }
        WAITL0();
#pragma unroll
        for (int s = 0; s < 4; ++s) qf[qb][s] = lds_rd16(rq_l + s * 512);
        WAITL0();
      }
    }
    BARRIER();
    f32x4 O[3][8];
#pragma unroll
    for (int d = 0; d < 8; ++d) { O[0][d] = f32x4{0, 0, 0, 0}; O[1][d] = f32x4{0, 0, 0, 0}; O[2][d] = f32x4{0, 0, 0, 0}; }
    f32x4 L[3] = {f32x4{0, 0, 0, 0}, f32x4{0, 0, 0, 0}, f32x4{0, 0, 0, 0}};
    const bf16x8 ones = {0x3F80, 0x3F80, 0x3F80, 0x3F80, 0x3F80, 0x3F80, 0x3F80, 0x3F80};
    f32x4 negm[3] = {f32x4{0, 0, 0, 0}, f32x4{0, 0, 0, 0}, f32x4{0, 0, 0, 0}};
    unsigned koff[4];
#pragma unroll
    for (int s = 0; s < 4; ++s) koff[s] = lb + 256u * l15 + 16u * ((unsigned)(4 * s + g) ^ (unsigned)l15);
    const unsigned vbase = lb + 256u * (4 * g + q4) + 8u * (p4 & 1) + 16u * (p4 >> 1) + 32u * (unsigned)(4 * (g & 1) + q4);
    f32x4 S[3][4];
    bf16x8 P[3][2];
    int slot = 0;
    for (int t = 0; t < 8; ++t) {
      BARRIER();
      {
        const unsigned ka = slot * 16384;
        bf16x8 F[PFD];
#pragma unroll
        for (int f = 0; f < PFD; ++f) F[f] = lds_rd16(koff[f & 3] + ka + (f >> 2) * 4096);
#pragma unroll
        for (int f = 0; f < 16; ++f) {
          const int kb = f >> 2, s = f & 3;
          const bf16x8 A = F[f % PFD];
          if (f + PFD < 16) F[f % PFD] = lds_rd16(koff[(f + PFD) & 3] + ka + ((f + PFD) >> 2) * 4096);
#pragma unroll
          for (int qb = 0; qb < 3; ++qb) S[qb][kb] = mfma16(A, qf[qb][s], s == 0 ? negm[qb] : S[qb][kb]);
          __builtin_amdgcn_sched_barrier(0);
        }
        float mx[3];
#pragma unroll
        for (int qb = 0; qb < 3; ++qb) {
          float m = S[qb][0][0];
#pragma unroll
          for (int kb = 0; kb < 4; ++kb)
#pragma unroll
            for (int r = 0; r < 4; ++r) m = fmaxf(m, S[qb][kb][r]);
          mx[qb] = xmax_groups(m);
        }
        const bool need = (t == 0) || (mx[0] > THR) || (mx[1] > THR) || (mx[2] > THR);
        if (__any(need)) {
#pragma unroll
          for (int qb = 0; qb < 3; ++qb) {
            const float dl = (t == 0) ? mx[qb] : fmaxf(mx[qb], 0.f);
            const float al = (t == 0) ? 1.f : __builtin_amdgcn_exp2f(-dl);
            negm[qb] -= dl; L[qb] *= al;
#pragma unroll
            for (int kb = 0; kb < 4; ++kb) S[qb][kb] -= dl;
#pragma unroll
            for (int d = 0; d < 8; ++d) O[qb][d] *= al;
          }
        }
#pragma unroll
        for (int qb = 0; qb < 3; ++qb) {
#pragma unroll
          for (int kb = 0; kb < 4; ++kb)
#pragma unroll
            for (int r = 0; r < 4; ++r) S[qb][kb][r] = __builtin_amdgcn_exp2f(S[qb][kb][r]);
#pragma unroll
          for (int jj = 0; jj < 2; ++jj) {
            u32x4 w = {cvtpk(S[qb][2 * jj][0], S[qb][2 * jj][1]), cvtpk(S[qb][2 * jj][2], S[qb][2 * jj][3]),
                       cvtpk(S[qb][2 * jj + 1][0], S[qb][2 * jj + 1][1]), cvtpk(S[qb][2 * jj + 1][2], S[qb][2 * jj + 1][3])};
            P[qb][jj] = __builtin_bit_cast(bf16x8, w);
            L[qb] = mfma16(ones, P[qb][jj], L[qb]);
          }
        }
      }
      slot = slot == 4 ? 0 : slot + 1;
      { bf16x8 &p0 = P[0][0], &p1 = P[0][1], &p2 = P[1][0], &p3 = P[1][1], &p4_ = P[2][0], &p5 = P[2][1];
        asm volatile("" : "+v"(p0), "+v"(p1), "+v"(p2), "+v"(p3), "+v"(p4_), "+v"(p5)); }
      BARRIER();
      {
        const unsigned va = vbase + slot * 16384;
        bf16x8 F[PFD];
#pragma unroll
        for (int f = 0; f < PFD; ++f) { const unsigned a0 = (va ^ (32u * (f & 7))) + (f >> 3) * 8192; F[f] = lds_tr2(a0, a0 + 4096); }
#pragma unroll
        for (int f = 0; f < 16; ++f) {
          const int jj = f >> 3, d = f & 7;
          const bf16x8 A = F[f % PFD];
          if (f + PFD < 16) { const unsigned a0 = (va ^ (32u * ((f + PFD) & 7))) + ((f + PFD) >> 3) * 8192; F[f % PFD] = lds_tr2(a0, a0 + 4096); }
#pragma unroll
          for (int qb = 0; qb < 3; ++qb) O[qb][d] = mfma16(A, P[qb][jj], O[qb][d]);
          __builtin_amdgcn_sched_barrier(0);
        }
      }
      slot = slot == 4 ? 0 : slot + 1;
    }
    BARRIER();
    {
      int tz = threadIdx.x; asm volatile("" : "+v"(tz));
      const int lane = tz & 63, l15 = lane & 15, g = lane >> 4;
#pragma unroll
      for (int qb = 0; qb < 3; ++qb) {
        const float il = 1.0f / L[qb][0];
        const unsigned row = (unsigned)((3 * wid + qb) * 16 + l15);
#pragma unroll
        for (int d = 0; d < 8; ++d) {
          u32x2 w = {cvtpk(O[qb][d][0] * il, O[qb][d][1] * il), cvtpk(O[qb][d][2] * il, O[qb][d][3] * il)};
          *lp<lds_u32x2>(lb + 256u * row + 16u * ((unsigned)(2 * d + (g >> 1)) ^ l15) + 8u * (g & 1)) = w;
        }
      }
    }
    BARRIER();
  }
  {
    int tz = threadIdx.x; asm volatile("" : "+v"(tz));
    u16* og = o_ws + ((size_t)(br * 64 + 2 * h) * MROWS + (size_t)b * SQ) * 64;
#pragma unroll
    for (int i = 0; i < 9; ++i) {
      const unsigned row = (unsigned)(i * 16 + (tz >> 4)), c = tz & 15;
      u32x4 v = *lp<lds_u32x4>(lb + 256u * row + 16u * (c ^ (row & 15)));
      *reinterpret_cast<u32x4*>(og + (size_t)(c >> 3) * MROWS * 64 + (size_t)row * 64 + (c & 7) * 8) = v;
    }
  }
}

constexpr int K2_STAGE = 40960, K2_LDS = K2_STAGE * 4;

__global__ __launch_bounds__(512, 1)
void k_oproj(const u16* __restrict__ o_ws, const u16* __restrict__ woA, const u16* __restrict__ woB, u16* __restrict__ out) {
  extern __shared__ __attribute__((aligned(16))) char smem[];
  const unsigned lb = (unsigned)(uintptr_t)smem;
  const int tid = threadIdx.x, lane = tid & 63;
  const int wv = __builtin_amdgcn_readfirstlane(tid >> 6), wid = wv & 3;
  const int bid = blockIdx.x;
  const int xcd = bid & 7, idx = bid >> 3;
  const int br = xcd >> 2, mt = (xcd & 3) * 3 + idx / 10, nt = idx % 10;
  const int l15 = lane & 15, g = lane >> 4, q4 = l15 >> 2, p4 = lane & 3;
  if (wv >= 4) {
    const int rot = ((xcd & 3) * 8) & 31;
    const rsrc_t rO = mkrsrc(o_ws + ((size_t)br * 64 * MROWS + (size_t)mt * 96) * 64, (unsigned)((64 * MROWS - mt * 96) * 128));
    const rsrc_t rW = mkrsrc((br ? woB : woA) + nt * 64, (unsigned)(HD * DM * 2 - nt * 128));
    unsigned ovo[2], wvo[2];
#pragma unroll
    for (int p = 0; p < 2; ++p) {
      ovo[p] = (unsigned)((lane >> 3) * 128 + (((lane & 7) ^ (4 * p + (lane >> 4))) * 16));
      wvo[p] = (unsigned)((lane >> 3) * (DM * 2) + (((lane & 7) ^ ((((lane >> 4) & 1) | (p << 1)) << 1)) * 16));
    }
    const unsigned ova = (wid & 1) ? ovo[1] : ovo[0], ovb = (wid & 1) ? ovo[0] : ovo[1];
    auto load_stage = [&](int c0) {
      const unsigned sb = lb + (c0 & 3) * K2_STAGE;
      const int c = (c0 + rot) & 31;
      dma3(rO, sb + wid * 3072, ova, ovb, (unsigned)(2 * c) * (MROWS * 128) + wid * 3072);
      dma3(rO, sb + 12288 + wid * 3072, ova, ovb, (unsigned)(2 * c + 1) * (MROWS * 128) + wid * 3072);
      const unsigned so = (unsigned)(c * 128 + 32 * wid) * (DM * 2);
      dma4d(rW, sb + 24576 + wid * 4096, wvo[0], wvo[1], so, so + 8 * (DM * 2), so + 16 * (DM * 2), so + 24 * (DM * 2));
    };
    load_stage(0); load_stage(1); load_stage(2);
    for (int c = 0; c < 32; ++c) {
      if (c < 30) waitv<20>(); else if (c < 31) waitv<10>(); else waitv<0>();
      BARRIER();
      if (c + 3 < 32) load_stage(c + 3);
    }
    BARRIER();
    BARRIER();
  } else {
    const int nh = wid & 1, kh = wid >> 1;
    f32x4 acc[2][6];
#pragma unroll
    for (int n = 0; n < 2; ++n)
#pragma unroll
      for (int m = 0; m < 6; ++m) acc[n][m] = f32x4{0, 0, 0, 0};
    const unsigned boff = (unsigned)(128 * l15 + 16 * ((4 * kh + g) ^ ((l15 >> 1) & 7)));
    unsigned aoff[2];
#pragma unroll
    for (int n = 0; n < 2; ++n) {
      const unsigned fl = (unsigned)(((q4 >> 1) & 1) | ((g & 1) << 1));
      aoff[n] = 24576u + 128u * (32 * kh + 8 * g + q4) + 32u * ((unsigned)(2 * nh + n) ^ fl) + 16u * (p4 >> 1) + 8u * (p4 & 1);
    }
    for (int c = 0; c < 32; ++c) {
      BARRIER();
      const unsigned sb = lb + (c & 3) * K2_STAGE;
      bf16x8 Bf[2][6], A[2][2];
#pragma unroll
      for (int sc = 0; sc < 2; ++sc) {
#pragma unroll
        for (int n = 0; n < 2; ++n) { const unsigned a0 = sb + aoff[n] + sc * 8192; A[sc][n] = lds_tr2(a0, a0 + 512); }
#pragma unroll
        for (int m = 0; m < 6; ++m) Bf[sc][m] = lds_rd16(sb + sc * 12288 + boff + m * 2048);
      }
#pragma unroll
      for (int sc = 0; sc < 2; ++sc)
#pragma unroll
        for (int n = 0; n < 2; ++n)
#pragma unroll
          for (int m = 0; m < 6; ++m) acc[n][m] = mfma16(A[sc][n], Bf[sc][m], acc[n][m]);
    }
    BARRIER();
    if (kh == 1) {
#pragma unroll
      for (int n = 0; n < 2; ++n)
#pragma unroll
        for (int m = 0; m < 6; ++m) *lp<lds_f32x4>(lb + (unsigned)(((nh * 12) + n * 6 + m) * 1024) + lane * 16) = acc[n][m];
    }
    BARRIER();
    if (kh == 0) {
      u16* og = out + (size_t)(mt * 96) * (2 * DM) + br * DM + nt * 64;
#pragma unroll
      for (int n = 0; n < 2; ++n)
#pragma unroll
        for (int m = 0; m < 6; ++m) {
          f32x4 v = acc[n][m] + *lp<lds_f32x4>(lb + (unsigned)(((nh * 12) + n * 6 + m) * 1024) + lane * 16);
          u32x2 w = {cvtpk(v[0], v[1]), cvtpk(v[2], v[3])};
          *reinterpret_cast<u32x2*>(og + (size_t)(m * 16 + l15) * (2 * DM) + (2 * nh + n) * 16 + 4 * g) = w;
        }
    }
  }
}

extern "C" void kernel_launch(void* const* d_in, const int* in_sizes, int n_in,
                              void* d_out, int out_size, void* d_ws, size_t ws_size,
                              hipStream_t stream) {
  const u16* xA = (const u16*)d_in[0];
  const u16* kA = (const u16*)d_in[1];
  const u16* vA = (const u16*)d_in[2];
  const u16* wqA = (const u16*)d_in[3];
  const u16* woA = (const u16*)d_in[4];
  const u16* xB = (const u16*)d_in[5];
  const u16* kB = (const u16*)d_in[6];
  const u16* vB = (const u16*)d_in[7];
  const u16* wqB = (const u16*)d_in[8];
  const u16* woB = (const u16*)d_in[9];
  u16* o_ws = (u16*)d_ws;
  (void)hipFuncSetAttribute((const void*)k_attn, hipFuncAttributeMaxDynamicSharedMemorySize, K1_LDS);
  (void)hipFuncSetAttribute((const void*)k_oproj, hipFuncAttributeMaxDynamicSharedMemorySize, K2_LDS);
  k_attn<<<512, 256, K1_LDS, stream>>>(xA, kA, vA, wqA, xB, kB, vB, wqB, woA, woB, o_ws);
  k_oproj<<<240, 512, K2_LDS, stream>>>(o_ws, woA, woB, (u16*)d_out);
}
```
